```python
import torch

def _softmax(x, axis=-1):
    return torch.softmax(x, dim=axis)
def _gelu(x):
    return torch.nn.functional.gelu(x, approximate="tanh")
def _silu(x):
    return torch.nn.functional.silu(x)
def _sigmoid(x):
    return torch.sigmoid(x)
def _layernorm(x, axis=-1, eps=1e-5):
    m = x.mean(dim=axis, keepdim=True)
    v = x.var(dim=axis, keepdim=True, unbiased=False)
    return (x - m) / torch.sqrt(v + eps)
def _scatter_add(tgt, idx, src, axis=0):
    return tgt.clone().index_add_(axis, idx, src)
def _segment_sum(src, seg, n):
    out = torch.zeros((n,) + src.shape[1:], dtype=src.dtype)
    return out.index_add_(0, seg, src)
def _pad(x, pw):
    flat = tuple(v for pair in reversed(pw) for v in pair)
    return torch.nn.functional.pad(x, flat)

kernel_name = 'parallel_sum__sliding_local_dilated__glu_gated_glu__prompt'

def setup_inputs():

    _w = (lambda a: (lambda t: t.to(torch.bfloat16) if t.is_floating_point() else t)(torch.as_tensor(a)))
    def _A_setup():
        B, S, D = 1, 2048, 640
        return {
            "x": _w((lambda *s: torch.randn(*s, dtype=torch.bfloat16) * 0.05)(B, S, D)),
            "wqkv": _w((lambda *s: torch.randn(*s, dtype=torch.bfloat16) * 0.05)(D, 3 * D)),
            "wo": _w((lambda *s: torch.randn(*s, dtype=torch.bfloat16) * 0.05)(D, D)),
        }
    _A_in = _A_setup()

    def _B_setup():
        B, S, D, F = 1, 2048, 640, 1536
        return {
            "x": _w((lambda *s: torch.randn(*s, dtype=torch.bfloat16) * 0.05)(B, S, D)),
            "w_gate": _w((lambda *s: torch.randn(*s, dtype=torch.bfloat16) * 0.05)(D, F)),
            "w_up": _w((lambda *s: torch.randn(*s, dtype=torch.bfloat16) * 0.05)(D, F)),
            "w_down": _w((lambda *s: torch.randn(*s, dtype=torch.bfloat16) * 0.05)(F, D)),
        }
    _B_in = _B_setup()

    return {
        "A_x": _A_in["x"],
        "A_wqkv": _A_in["wqkv"],
        "A_wo": _A_in["wo"],
        "B_x": _B_in["x"],
        "B_w_gate": _B_in["w_gate"],
        "B_w_up": _B_in["w_up"],
        "B_w_down": _B_in["w_down"],
    }

def reference(A_x, A_wqkv, A_wo, B_x, B_w_gate, B_w_up, B_w_down):
    def _A_ref(x, wqkv, wo):
        B, S, D = x.shape
        H = 32; Dh = D // H
        W, dil = 8, 2
        qkv = (lambda x, *d: x.permute(*d))((x @ wqkv).reshape(B, S, 3, H, Dh), 2, 0, 3, 1, 4)
        q, k, v = qkv[0], qkv[1], qkv[2]
        scale = Dh ** -0.5
        pos = torch.arange(S)
        off = torch.arange(W) * dil
        idx = pos[:, None] - off[None, :]
        mask = idx >= 0
        idx_c = torch.where(mask, idx, 0).reshape(-1)
        kg = k[:, :, idx_c, :].reshape(B, H, S, W, Dh)
        vg = v[:, :, idx_c, :].reshape(B, H, S, W, Dh)
        logits = torch.einsum("bhsd,bhswd->bhsw", q, kg) * scale
        logits = torch.where(mask[None, None, :, :], logits, -1e9)
        attn = _softmax(logits, axis=-1)
        o = torch.einsum("bhsw,bhswd->bhsd", attn, vg)
        return (lambda x, *d: x.permute(*d))(o, 0, 2, 1, 3).reshape(B, S, D) @ wo

    def _B_ref(x, w_gate, w_up, w_down):
        g = torch.einsum("bsd,df->bsf", x, w_gate)
        u = torch.einsum("bsd,df->bsf", x, w_up)
        h = _sigmoid(g) * u
        return torch.einsum("bsf,fd->bsd", h, w_down)

    x = A_x
    xn = _layernorm(x, axis=-1)
    return x + _A_ref(xn, A_wqkv, A_wo) + _B_ref(xn, B_w_gate, B_w_up, B_w_down)
```

```cpp
#include <hip/hip_runtime.h>
#include <hip/hip_bf16.h>
#include <stdint.h>

typedef short v4s __attribute__((ext_vector_type(4)));
typedef __bf16 bf8 __attribute__((ext_vector_type(8)));
typedef __bf16 bf2 __attribute__((ext_vector_type(2)));
typedef float f4 __attribute__((ext_vector_type(4)));
typedef unsigned u4 __attribute__((ext_vector_type(4)));
typedef unsigned u2 __attribute__((ext_vector_type(2)));
typedef unsigned short bf16_t;

#define LAS __attribute__((address_space(3)))
#define GAS __attribute__((address_space(1)))
#define DEVI __device__ __forceinline__

__device__ const __attribute__((aligned(16))) unsigned g_ones[4] = {0x3F803F80u, 0x3F803F80u, 0x3F803F80u, 0x3F803F80u};

DEVI float bfr(float v) { return (float)(__bf16)v; }
DEVI unsigned pk2(float a, float b) { bf2 c; c[0] = (__bf16)a; c[1] = (__bf16)b; return __builtin_bit_cast(unsigned, c); }
DEVI float lo16(unsigned v) { return __builtin_bit_cast(float, v << 16); }
DEVI float hi16(unsigned v) { return __builtin_bit_cast(float, v & 0xffff0000u); }
DEVI float dot2(unsigned a, unsigned b, float c) {
  return __builtin_amdgcn_fdot2_f32_bf16(__builtin_bit_cast(bf2, a), __builtin_bit_cast(bf2, b), c, false);
}
DEVI void glds16(const void* g, unsigned ldsoff, char* lds) {
  __builtin_amdgcn_global_load_lds((const GAS void*)g, (LAS void*)(lds + ldsoff), 16, 0, 0);
}
#define RD128(d, addr, off) asm volatile("ds_read_b128 %0, %1 offset:%2" : "=v"(d) : "v"(addr), "n"(off))
#define RDTR(d, addr, off) asm volatile("ds_read_b64_tr_b16 %0, %1 offset:%2" : "=v"(d) : "v"(addr), "n"(off))
DEVI bf8 cat2(u2 a, u2 b) { u4 r; r[0] = a[0]; r[1] = a[1]; r[2] = b[0]; r[3] = b[1]; return __builtin_bit_cast(bf8, r); }
DEVI bf8 asbf(u4 a) { return __builtin_bit_cast(bf8, a); }
#define MFMA(a, b, c) __builtin_amdgcn_mfma_f32_16x16x32_bf16(a, b, c, 0, 0, 0)
#define WAITV(n) asm volatile("s_waitcnt vmcnt(" #n ")" ::: "memory")
#define WAITL0() asm volatile("s_waitcnt lgkmcnt(0)" ::: "memory")

constexpr int SLOT1 = 29696;
constexpr int IMG_STRIDE = 648;
constexpr int STAT_OFF = 144 * IMG_STRIDE;

template <int N> DEVI void wait_groups() {
  if constexpr (N == 0) WAITV(0); else if constexpr (N == 1) WAITV(4); else if constexpr (N == 2) WAITV(8);
  else if constexpr (N == 3) WAITV(12); else WAITV(16);
}

struct K1Ctx {
  const char* src[4];
  unsigned str[4];
  unsigned pdst[4];
  unsigned smul;
};

DEVI void k1_issue(K1Ctx& c, char* lds, int slot) {
#pragma unroll
  for (int i = 0; i < 3; ++i) { glds16(c.src[i], slot * SLOT1 + c.pdst[i], lds); c.src[i] += c.str[i]; }
  glds16(c.src[3], slot * c.smul + c.pdst[3], lds); c.src[3] += c.str[3];
}

template <int MH>
DEVI void k1_main(K1Ctx& c, char* lds, int lane, int j, float* __restrict__ dummy) {
  constexpr int NRT = MH == 0 ? 5 : 4;
  constexpr int RT0 = MH == 0 ? 0 : 5;
  constexpr int NG = MH == 0 ? 1 : 2;
  constexpr int NA = NRT + (MH == 0 ? 0 : 2);
  f4 acc[NRT][5];
  f4 gacc[NG];
#pragma unroll
  for (int r = 0; r < NRT; ++r)
#pragma unroll
    for (int q = 0; q < 5; ++q) acc[r][q] = f4{0.f, 0.f, 0.f, 0.f};
#pragma unroll
  for (int r = 0; r < NG; ++r) gacc[r] = f4{0.f, 0.f, 0.f, 0.f};
  const unsigned lbase = (unsigned)(uintptr_t)(LAS char*)lds;
  const int ar = lane & 15, ac = lane >> 4;
  const unsigned abase = lbase + ar * 64 + ((ac ^ (((ar >> 3) & 1) << 1)) * 16);
  const unsigned ag0 = abase + j * 1024;
  const int g = lane >> 4, q4 = (lane & 15) >> 2, p4 = lane & 3;
  const unsigned blp = lbase + 9216 + g * 512 + q4 * 64 + 8 * p4;
  const unsigned bE = blp + (((j & 1) ^ (g & 1)) * 32) + ((5 * j) >> 1) * 2048;
  const unsigned bO = blp + ((((j + 1) & 1) ^ (g & 1)) * 32) + (((5 * j) >> 1) + (j & 1)) * 2048;
  u4 fa[2][NA];
  u2 fb[2][5][2];
  auto rd = [&](int buf, int slot) {
    const unsigned so = slot * SLOT1;
    const unsigned a0 = abase + so, e0 = bE + so, o0 = bO + so;
    RDTR(fb[buf][0][0], e0, 0);    RDTR(fb[buf][0][1], e0, 256);
#pragma unroll
    for (int r = 0; r < NRT; ++r) RD128(fa[buf][r], a0, (RT0 + r) * 1024);
    RDTR(fb[buf][1][0], o0, 0);    RDTR(fb[buf][1][1], o0, 256);
    RDTR(fb[buf][2][0], e0, 2048); RDTR(fb[buf][2][1], e0, 2304);
    RDTR(fb[buf][3][0], o0, 2048); RDTR(fb[buf][3][1], o0, 2304);
    RDTR(fb[buf][4][0], e0, 4096); RDTR(fb[buf][4][1], e0, 4352);
    if constexpr (MH == 1) { const unsigned g0 = ag0 + so; RD128(fa[buf][NRT], g0, 0); RD128(fa[buf][NRT + 1], g0, 5120); }
  };
  auto wait_frags = [&](int buf) {
    asm volatile("s_waitcnt lgkmcnt(0)"
                 : "+v"(fa[buf][0]), "+v"(fa[buf][1]), "+v"(fa[buf][2]), "+v"(fa[buf][3]), "+v"(fa[buf][4]),
                   "+v"(fb[buf][0][0]), "+v"(fb[buf][0][1]), "+v"(fb[buf][1][0]), "+v"(fb[buf][1][1]), "+v"(fb[buf][2][0]),
                   "+v"(fb[buf][2][1]), "+v"(fb[buf][3][0]), "+v"(fb[buf][3][1]), "+v"(fb[buf][4][0]), "+v"(fb[buf][4][1])
                 :: "memory");
    if constexpr (MH == 1) asm volatile("" : "+v"(fa[buf][NA - 1]) :: "memory");
  };
#pragma unroll
  for (int s = 0; s < 5; ++s) k1_issue(c, lds, s);
  wait_groups<4>();
  __builtin_amdgcn_s_barrier();
  rd(0, 0);
#pragma unroll
  for (int s = 0; s < 20; ++s) {
    if (s <= 15) wait_groups<3>();
    else if (s == 16) wait_groups<2>();
    else if (s == 17) wait_groups<1>();
    else if (s == 18) wait_groups<0>();
    const int b = s & 1;
    wait_frags(b);
    __builtin_amdgcn_s_barrier();
    if (s + 5 < 20) k1_issue(c, lds, s % 5);
    if (s + 1 < 20) rd((s + 1) & 1, (s + 1) % 5);
#pragma unroll
    for (int ci = 0; ci < 5; ++ci)
#pragma unroll
      for (int r = 0; r < NRT; ++r) acc[r][ci] = MFMA(cat2(fb[b][ci][0], fb[b][ci][1]), asbf(fa[b][r]), acc[r][ci]);
    if constexpr (MH == 0) gacc[0] = MFMA(asbf(fa[b][4]), asbf(fa[b][4]), gacc[0]);
    else {
      gacc[0] = MFMA(asbf(fa[b][NRT]), asbf(fa[b][NRT]), gacc[0]);
      gacc[1] = MFMA(asbf(fa[b][NRT + 1]), asbf(fa[b][NRT + 1]), gacc[1]);
    }
  }
  float* st_rs = (float*)(lds + STAT_OFF);
  float* st_sq = st_rs + 144;
  float* st_cs = st_sq + 144;
  const bool diag = ((lane & 15) >> 2) == (lane >> 4);
  {
    const int rr = lane & 3;
    if constexpr (MH == 0) {
      float v = rr == 0 ? gacc[0][0] : rr == 1 ? gacc[0][1] : rr == 2 ? gacc[0][2] : gacc[0][3];
      if (diag && j == 0) st_sq[64 + (lane & 15)] = v;
    } else {
      float v0 = rr == 0 ? gacc[0][0] : rr == 1 ? gacc[0][1] : rr == 2 ? gacc[0][2] : gacc[0][3];
      float v1 = rr == 0 ? gacc[1][0] : rr == 1 ? gacc[1][1] : rr == 2 ? gacc[1][2] : gacc[1][3];
      if (diag) { st_sq[16 * j + (lane & 15)] = v0; st_sq[16 * (5 + j) + (lane & 15)] = v1; }
    }
  }
  if (j == 1 && (lane >> 4) == 2) {
#pragma unroll
    for (int r = 0; r < NRT; ++r) st_rs[16 * (RT0 + r) + (lane & 15)] = acc[r][2][0];
  }
  if constexpr (MH == 0) {
    if ((lane & 15) == 0) {
#pragma unroll
      for (int ci = 0; ci < 5; ++ci) *(f4*)(st_cs + 16 * (5 * j + ci) + 4 * (lane >> 4)) = acc[0][ci];
    }
  }
  __syncthreads();
  f4 cs[5];
#pragma unroll
  for (int ci = 0; ci < 5; ++ci) cs[ci] = *(const f4*)(st_cs + 16 * (5 * j + ci) + 4 * (lane >> 4));
#pragma unroll
  for (int r = 0; r < NRT; ++r) {
    const int row = 16 * (RT0 + r) + (lane & 15);
    const float mean = st_rs[row] * (1.0f / 640.0f);
    float var = st_sq[row] * (1.0f / 640.0f) - mean * mean;
    var = fmaxf(var, 0.f);
    const float m = bfr(mean);
    const float sd = bfr(sqrtf(bfr(bfr(var) + 1e-5f)));
    const float rinv = 1.0f / sd;
#pragma unroll
    for (int ci = 0; ci < 5; ++ci) {
      f4 y = (acc[r][ci] - m * cs[ci]) * rinv;
      u2 o; o[0] = pk2(y[0], y[1]); o[1] = pk2(y[2], y[3]);
      *(u2*)(lds + row * IMG_STRIDE + (16 * (5 * j + ci) + 4 * (lane >> 4)) * 2) = o;
    }
  }
}

DEVI unsigned a2_off(int grow, int kcol) {
  return (((((unsigned)grow >> 6) * 68 + ((unsigned)kcol >> 5)) * 4 + (((unsigned)grow >> 4) & 3)) * 64 +
          (((((unsigned)kcol >> 3) & 3) << 4) | ((unsigned)grow & 15))) * 8 + ((unsigned)kcol & 7);
}

DEVI void glu_task(const char* lds, int T, int r0, int cg, bf16_t* __restrict__ A2) {
  const int row = T & 127, c = T >> 7;
  const char* p = lds + (16 + row) * IMG_STRIDE + 256 + 16 * c;
  u2 g0 = *(const u2*)p, g1 = *(const u2*)(p + 8), u0 = *(const u2*)(p + 192), u1 = *(const u2*)(p + 200);
  unsigned gg[4] = {g0[0], g0[1], g1[0], g1[1]}, uu[4] = {u0[0], u0[1], u1[0], u1[1]};
  u4 o;
#pragma unroll
  for (int i = 0; i < 4; ++i) {
    float s0 = bfr(__builtin_amdgcn_rcpf(1.0f + __expf(-lo16(gg[i]))));
    float s1 = bfr(__builtin_amdgcn_rcpf(1.0f + __expf(-hi16(gg[i]))));
    o[i] = pk2(s0 * lo16(uu[i]), s1 * hi16(uu[i]));
  }
  *(u4*)(A2 + a2_off(r0 + row, 640 + 96 * cg + 8 * c)) = o;
}

__global__ __launch_bounds__(512, 2) void k1_kernel(const bf16_t* __restrict__ x, const bf16_t* __restrict__ wqkv,
                                                    const bf16_t* __restrict__ wg, const bf16_t* __restrict__ wu,
                                                    bf16_t* __restrict__ A2) {
  __shared__ __attribute__((aligned(16))) char lds[5 * SLOT1 + 1024];
  const int tid = threadIdx.x, lane = tid & 63;
  const int w = __builtin_amdgcn_readfirstlane(tid >> 6);
  const int bid = blockIdx.x, xcd = bid & 7, idx = bid >> 3;
  const int rb = 8 * (xcd & 1) + (idx & 7), cg = 4 * (xcd >> 1) + (idx >> 3);
  const int r0 = rb * 128;
  K1Ctx c;
  c.smul = ((w >= 4) || (w == 0)) ? SLOT1 : 0;
#pragma unroll
  for (int i = 0; i < 4; ++i) {
    int p = (w >= 4) ? (w - 4) + 4 * i : 16 + w + 4 * i;
    c.pdst[i] = p * 1024;
    const int lr = lane >> 2, cpos = lane & 3;
    if (p > 28) {
      c.pdst[i] = 5 * SLOT1; c.src[i] = (const char*)g_ones; c.str[i] = 0;
    } else if (p < 9) {
      const int chunk = cpos ^ (((lr >> 3) & 1) << 1);
      int grow = r0 - 16 + 16 * p + lr;
      grow = grow < 0 ? 0 : grow;
      const bool ones = (p == 0 && lr == 0);
      c.src[i] = ones ? (const char*)g_ones : (const char*)(x + grow * 640 + chunk * 8);
      c.str[i] = ones ? 0u : 64u;
    } else {
      const int q = p - 9, blk = q >> 1, k = 16 * (q & 1) + lr;
      const int ch = blk * 4 + (cpos ^ (((k >> 3) & 1) << 1));
      if (ch < 15) {
        c.src[i] = (const char*)(wqkv + k * 1920 + (ch / 5) * 640 + 40 * cg + 8 * (ch % 5));
        c.str[i] = 32 * 1920 * 2;
      } else if (ch == 15) {
        c.src[i] = (const char*)g_ones; c.str[i] = 0;
      } else if (ch < 28) {
        c.src[i] = (const char*)(wg + k * 1536 + 96 * cg + 8 * (ch - 16));
        c.str[i] = 32 * 1536 * 2;
      } else {
        c.src[i] = (const char*)(wu + k * 1536 + 96 * cg + 8 * (ch - 28));
        c.str[i] = 32 * 1536 * 2;
      }
    }
  }
  if (w < 4) k1_main<0>(c, lds, lane, w & 3, nullptr);
  else k1_main<1>(c, lds, lane, w & 3, nullptr);
  __syncthreads();
  if (w < 4) {
    const int hh = tid >> 7, row = tid & 127, pos = r0 + row;
    const char* pr = lds + (16 + row) * IMG_STRIDE + 40 * hh;
    unsigned qv[10];
#pragma unroll
    for (int i = 0; i < 5; ++i) { u2 t = *(const u2*)(pr + 8 * i); qv[2 * i] = t[0]; qv[2 * i + 1] = t[1]; }
    float lg[8];
#pragma unroll
    for (int ww = 0; ww < 8; ++ww) {
      const char* pk = pr - 2 * ww * IMG_STRIDE + 80;
      float a = 0.f;
#pragma unroll
      for (int i = 0; i < 5; ++i) {
        u2 t = *(const u2*)(pk + 8 * i);
        a = dot2(qv[2 * i], t[0], a); a = dot2(qv[2 * i + 1], t[1], a);
      }
      a = bfr(bfr(a) * 0.22360679774997896f);
      lg[ww] = (pos - 2 * ww >= 0) ? a : -1e9f;
    }
    float mx = lg[0];
#pragma unroll
    for (int ww = 1; ww < 8; ++ww) mx = fmaxf(mx, lg[ww]);
    float sum = 0.f;
#pragma unroll
    for (int ww = 0; ww < 8; ++ww) { lg[ww] = __expf(lg[ww] - mx); sum += lg[ww]; }
    const float inv = 1.0f / sum;
    float o[20];
#pragma unroll
    for (int i = 0; i < 20; ++i) o[i] = 0.f;
#pragma unroll
    for (int wp = 0; wp < 4; ++wp) {
      const unsigned P = pk2(lg[2 * wp] * inv, lg[2 * wp + 1] * inv);
      const char* pv0 = pr - 4 * wp * IMG_STRIDE + 160;
      const char* pv1 = pv0 - 2 * IMG_STRIDE;
#pragma unroll
      for (int i = 0; i < 5; ++i) {
        u2 a = *(const u2*)(pv0 + 8 * i), b = *(const u2*)(pv1 + 8 * i);
#pragma unroll
        for (int e = 0; e < 2; ++e) {
          unsigned lo = __builtin_amdgcn_perm(b[e], a[e], 0x05040100u);
          unsigned hi = __builtin_amdgcn_perm(b[e], a[e], 0x07060302u);
          o[4 * i + 2 * e] = dot2(lo, P, o[4 * i + 2 * e]);
          o[4 * i + 2 * e + 1] = dot2(hi, P, o[4 * i + 2 * e + 1]);
        }
      }
    }
    const int kc0 = 40 * cg + 20 * hh;
#pragma unroll
    for (int i = 0; i < 5; ++i) {
      u2 t; t[0] = pk2(o[4 * i], o[4 * i + 1]); t[1] = pk2(o[4 * i + 2], o[4 * i + 3]);
      *(u2*)(A2 + a2_off(pos, kc0 + 4 * i)) = t;
    }
    glu_task(lds, tid, r0, cg, A2);
  } else {
#pragma unroll
    for (int i = 0; i < 5; ++i) glu_task(lds, tid + 256 * i, r0, cg, A2);
  }
}

constexpr int SLOT2 = 5120;

template <int NST>
DEVI void k2_main(const bf16_t* __restrict__ A2, const bf16_t* __restrict__ wo, const bf16_t* __restrict__ wd,
                  char* lds, int lane, int w, int rb64, int cb, f4 (&acc)[4][5]) {
  char* ring = lds + w * (3 * SLOT2);
  unsigned boff[5];
#pragma unroll
  for (int p = 0; p < 4; ++p) {
    const int k = 16 * (p & 1) + (lane >> 2), ch = (lane & 3) ^ (((k >> 3) & 1) << 1);
    boff[p] = (k * 640 + 80 * cb + 32 * (p >> 1) + 8 * ch) * 2;
  }
  {
    const int kp = lane >> 1, k = kp ^ (((kp >> 3) & 1) << 2);
    boff[4] = (k * 640 + 80 * cb + 64 + 8 * (lane & 1)) * 2;
  }
  const int g = lane >> 4, q4 = (lane & 15) >> 2, p4 = lane & 3;
  const unsigned rbase = (unsigned)(uintptr_t)(LAS char*)ring;
  const unsigned tb0 = rbase + g * 512 + q4 * 64 + ((g & 1) * 32) + 8 * p4;
  const unsigned tb1 = rbase + g * 512 + q4 * 64 + (((g & 1) ^ 1) * 32) + 8 * p4;
  const unsigned tc0 = rbase + 4096 + (8 * g + 4 * (g & 1) + q4) * 32 + 8 * p4;
  const unsigned tc1 = rbase + 4096 + (8 * g + 4 * ((g & 1) ^ 1) + q4) * 32 + 8 * p4;
  const char* abase = (const char*)(A2 + ((size_t)(rb64 * 68 + w) * 4 * 64 + lane) * 8);
  u4 fa[3][4];
  auto issue = [&](int i) {
    const int ks = w + 8 * i;
    const char* bp = ks < 20 ? (const char*)(wo + ks * 32 * 640) : (const char*)(wd + (ks - 20) * 32 * 640);
    const char* ap = abase + (size_t)i * 8 * 4096;
#pragma unroll
    for (int r = 0; r < 4; ++r)
      asm volatile("global_load_dwordx4 %0, %1, off offset:%2" : "=v"(fa[i % 3][r]) : "v"(ap), "n"(r * 1024));
#pragma unroll
    for (int p = 0; p < 5; ++p) glds16(bp + boff[p], (i % 3) * SLOT2 + p * 1024, ring);
  };
  issue(0);
  issue(1);
#pragma unroll
  for (int i = 0; i < NST; ++i) {
    const int b = i % 3;
    if (i + 1 < NST) asm volatile("s_waitcnt vmcnt(9)" : "+v"(fa[b][0]), "+v"(fa[b][1]), "+v"(fa[b][2]), "+v"(fa[b][3]) :: "memory");
    else asm volatile("s_waitcnt vmcnt(0)" : "+v"(fa[b][0]), "+v"(fa[b][1]), "+v"(fa[b][2]), "+v"(fa[b][3]) :: "memory");
    if (i + 2 < NST) issue(i + 2);
    u2 fb[5][2];
    RDTR(fb[0][0], tb0, b * SLOT2);        RDTR(fb[0][1], tb0, b * SLOT2 + 256);
    RDTR(fb[1][0], tb1, b * SLOT2);        RDTR(fb[1][1], tb1, b * SLOT2 + 256);
    RDTR(fb[2][0], tb0, b * SLOT2 + 2048); RDTR(fb[2][1], tb0, b * SLOT2 + 2304);
    RDTR(fb[3][0], tb1, b * SLOT2 + 2048); RDTR(fb[3][1], tb1, b * SLOT2 + 2304);
    RDTR(fb[4][0], tc0, b * SLOT2);        RDTR(fb[4][1], tc1, b * SLOT2);
    asm volatile("s_waitcnt lgkmcnt(6)" : "+v"(fb[0][0]), "+v"(fb[0][1]), "+v"(fb[1][0]), "+v"(fb[1][1]) :: "memory");
#pragma unroll
    for (int ci = 0; ci < 2; ++ci)
#pragma unroll
      for (int r = 0; r < 4; ++r) acc[r][ci] = MFMA(cat2(fb[ci][0], fb[ci][1]), asbf(fa[b][r]), acc[r][ci]);
    asm volatile("s_waitcnt lgkmcnt(0)" : "+v"(fb[2][0]), "+v"(fb[2][1]), "+v"(fb[3][0]), "+v"(fb[3][1]), "+v"(fb[4][0]), "+v"(fb[4][1]) :: "memory");
#pragma unroll
    for (int ci = 2; ci < 5; ++ci)
#pragma unroll
      for (int r = 0; r < 4; ++r) acc[r][ci] = MFMA(cat2(fb[ci][0], fb[ci][1]), asbf(fa[b][r]), acc[r][ci]);
  }
}

__global__ __launch_bounds__(512, 2) void k2_kernel(const bf16_t* __restrict__ A2, const bf16_t* __restrict__ wo,
                                                    const bf16_t* __restrict__ wd, const bf16_t* __restrict__ x,
                                                    bf16_t* __restrict__ out) {
  __shared__ __attribute__((aligned(16))) char lds[163840];
  const int tid = threadIdx.x, lane = tid & 63;
  const int w = __builtin_amdgcn_readfirstlane(tid >> 6);
  const int bid = blockIdx.x, xcd = bid & 7, idx = bid >> 3;
  const int rb64 = 8 * (xcd & 3) + (idx & 7), cb = 4 * (xcd >> 2) + (idx >> 3);
  f4 acc[4][5];
#pragma unroll
  for (int r = 0; r < 4; ++r)
#pragma unroll
    for (int q = 0; q < 5; ++q) acc[r][q] = f4{0.f, 0.f, 0.f, 0.f};
  if (w < 4) k2_main<9>(A2, wo, wd, lds, lane, w, rb64, cb, acc);
  else k2_main<8>(A2, wo, wd, lds, lane, w, rb64, cb, acc);
  __syncthreads();
#pragma unroll
  for (int r = 0; r < 4; ++r)
#pragma unroll
    for (int q = 0; q < 5; ++q) *(f4*)(lds + w * 20480 + (r * 5 + q) * 1024 + lane * 16) = acc[r][q];
  __syncthreads();
#pragma unroll
  for (int i = 0; i < 3; ++i) {
    const int P = tid + 512 * i;
    if (P < 1280) {
      f4 s = *(const f4*)(lds + P * 16);
#pragma unroll
      for (int ww = 1; ww < 8; ++ww) s += *(const f4*)(lds + ww * 20480 + P * 16);
      const int tile = P >> 6, ln = P & 63;
      const int rt = tile / 5, ci = tile % 5;
      const int grow = rb64 * 64 + 16 * rt + (ln & 15), gcol = 80 * cb + 16 * ci + 4 * (ln >> 4);
      u2 xv = *(const u2*)(x + grow * 640 + gcol);
      u2 o;
      o[0] = pk2(s[0] + lo16(xv[0]), s[1] + hi16(xv[0]));
      o[1] = pk2(s[2] + lo16(xv[1]), s[3] + hi16(xv[1]));
      *(u2*)(out + grow * 640 + gcol) = o;
    }
  }
}

extern "C" void kernel_launch(void* const* d_in, const int* in_sizes, int n_in, void* d_out, int out_size,
                              void* d_ws, size_t ws_size, hipStream_t stream) {
  const bf16_t* x = (const bf16_t*)d_in[0];
  const bf16_t* wqkv = (const bf16_t*)d_in[1];
  const bf16_t* wo = (const bf16_t*)d_in[2];
  const bf16_t* wg = (const bf16_t*)d_in[4];
  const bf16_t* wu = (const bf16_t*)d_in[5];
  const bf16_t* wd = (const bf16_t*)d_in[6];
  bf16_t* A2 = (bf16_t*)d_ws;
  k1_kernel<<<256, 512, 0, stream>>>(x, wqkv, wg, wu, A2);
  k2_kernel<<<256, 512, 0, stream>>>(A2, wo, wd, x, (bf16_t*)d_out);
}
```

```cpp
#include <hip/hip_runtime.h>
#include <hip/hip_bf16.h>
#include <stdint.h>

typedef short v4s __attribute__((ext_vector_type(4)));
typedef __bf16 bf8 __attribute__((ext_vector_type(8)));
typedef __bf16 bf2 __attribute__((ext_vector_type(2)));
typedef float f4 __attribute__((ext_vector_type(4)));
typedef unsigned u4 __attribute__((ext_vector_type(4)));
typedef unsigned u2 __attribute__((ext_vector_type(2)));
typedef unsigned short bf16_t;

#define LAS __attribute__((address_space(3)))
#define GAS __attribute__((address_space(1)))
#define DEVI __device__ __forceinline__

__device__ const __attribute__((aligned(16))) unsigned g_ones[4] = {0x3F803F80u, 0x3F803F80u, 0x3F803F80u, 0x3F803F80u};

DEVI float bfr(float v) { return (float)(__bf16)v; }
DEVI unsigned pk2(float a, float b) { bf2 c; c[0] = (__bf16)a; c[1] = (__bf16)b; return __builtin_bit_cast(unsigned, c); }
DEVI float lo16(unsigned v) { return __builtin_bit_cast(float, v << 16); }
DEVI float hi16(unsigned v) { return __builtin_bit_cast(float, v & 0xffff0000u); }
DEVI float dot2(unsigned a, unsigned b, float c) {
  return __builtin_amdgcn_fdot2_f32_bf16(__builtin_bit_cast(bf2, a), __builtin_bit_cast(bf2, b), c, false);
}
DEVI void glds16(const void* g, unsigned ldsoff, char* lds) {
  __builtin_amdgcn_global_load_lds((const GAS void*)g, (LAS void*)(lds + ldsoff), 16, 0, 0);
}
#define RD128(d, addr, off) asm volatile("ds_read_b128 %0, %1 offset:%2" : "=v"(d) : "v"(addr), "n"(off))
#define RDTR(d, addr, off) asm volatile("ds_read_b64_tr_b16 %0, %1 offset:%2" : "=v"(d) : "v"(addr), "n"(off))
DEVI bf8 cat2(u2 a, u2 b) { u4 r; r[0] = a[0]; r[1] = a[1]; r[2] = b[0]; r[3] = b[1]; return __builtin_bit_cast(bf8, r); }
DEVI bf8 asbf(u4 a) { return __builtin_bit_cast(bf8, a); }
#define MFMA(a, b, c) __builtin_amdgcn_mfma_f32_16x16x32_bf16(a, b, c, 0, 0, 0)
#define WAITV(n) asm volatile("s_waitcnt vmcnt(" #n ")" ::: "memory")
#define WAITL0() asm volatile("s_waitcnt lgkmcnt(0)" ::: "memory")

#define ST_SC
#define ST8(p, v) asm volatile("global_store_dwordx2 %0, %1, off sc0 sc1" :: "v"(p), "v"(v) : "memory")
#define ST8O(p, v, o) asm volatile("global_store_dwordx2 %0, %1, off offset:%2 sc0 sc1" :: "v"(p), "v"(v), "n"(o) : "memory")
#define ST16(p, v) asm volatile("global_store_dwordx4 %0, %1, off sc0 sc1" :: "v"(p), "v"(v) : "memory")
constexpr int ASLOT = 18432;
constexpr int BSLOT = 20480;
constexpr int BOFF = 3 * ASLOT;
constexpr int DUMMY_OFF = BOFF + 5 * BSLOT;
constexpr int IMG_STRIDE = 264;
constexpr int STAT_OFF = 144 * IMG_STRIDE;
#define STAMP(i)

DEVI unsigned a2_off(int grow, int kcol) {
  return (((((unsigned)grow >> 6) * 68 + ((unsigned)kcol >> 5)) * 4 + (((unsigned)grow >> 4) & 3)) * 64 +
          (((((unsigned)kcol >> 3) & 3) << 4) | ((unsigned)grow & 15))) * 8 + ((unsigned)kcol & 7);
}

struct K1Ctx {
  unsigned lo[5], hi[5];
  unsigned str[5];
  unsigned pdst[5];
  unsigned kind[5];
};

DEVI void k1_piece(K1Ctx& c, char* lds, int i, unsigned sb, unsigned sa, bool live, bool oddg) {
  const bool lv = (i == 2 && oddg) ? (live && c.kind[2] == 0) : live;
  const char* p = lv ? (const char*)(((uint64_t)c.hi[i] << 32) | c.lo[i]) : (const char*)g_ones;
  unsigned d;
  if (i < 2) d = sb + c.pdst[i];
  else if (i > 2) d = sa + c.pdst[i];
  else if (oddg) d = c.kind[2] == 0 ? sb + c.pdst[2] : (unsigned)DUMMY_OFF;
  else d = (c.kind[2] == 0 ? sb : c.kind[2] == 1 ? sa : 0u) + c.pdst[2];
  glds16(p, d, lds);
  asm("v_add_co_u32 %0, vcc, %0, %2\n\tv_addc_co_u32 %1, vcc, 0, %1, vcc" : "+v"(c.lo[i]), "+v"(c.hi[i]) : "v"(c.str[i]) : "vcc");
}
#define LBAR() do { asm volatile("s_waitcnt lgkmcnt(0)" ::: "memory"); __builtin_amdgcn_s_barrier(); asm volatile("" ::: "memory"); } while (0)

template <int MH, int NP>
DEVI void k1_main(K1Ctx& c, char* lds, int lane, int lst, int r0, int cg, bf16_t* __restrict__ A2,
                  unsigned long long* dbgp) {
  constexpr int NRT = MH == 0 ? 5 : 4;
  constexpr int RT0 = MH == 0 ? 0 : 5;
  constexpr int NC = 2 + 2 * NP;
  constexpr int NG = NP == 2 ? 0 : (MH == 0 ? 3 : 2);
  constexpr int NA = NRT + NG;
  f4 acc[NRT][NC];
  f4 gacc[NG + 1];
#pragma unroll
  for (int r = 0; r < NRT; ++r)
#pragma unroll
    for (int q = 0; q < NC; ++q) acc[r][q] = f4{0.f, 0.f, 0.f, 0.f};
#pragma unroll
  for (int r = 0; r < NG + 1; ++r) gacc[r] = f4{0.f, 0.f, 0.f, 0.f};
  const unsigned lbase = (unsigned)(uintptr_t)(LAS char*)lds;
  const int ar = lane & 15, ac = lane >> 4;
  const unsigned abase0 = lbase + ar * 128 + ((ac ^ (ar & 7)) * 16);
  const unsigned abase1 = lbase + ar * 128 + (((4 + ac) ^ (ar & 7)) * 16);
  const int gt0 = MH == 0 ? 3 * (lst - 2) : 6 + (lst - 2);
  const int g = lane >> 4, q4 = (lane & 15) >> 2, p4 = lane & 3;
  const int p0 = NP == 2 ? 2 * lst : lst + 2;
  const unsigned hx = (((q4 >> 1) & 1) | ((g & 1) << 1)) << 5;
  auto baddr = [&](int ct) { return lbase + BOFF + ((((8 * g + q4) * 640 + 32 * ct) ^ hx) + 8 * p4); };
  unsigned bb[NC];
  bb[0] = baddr(2 * lst); bb[1] = baddr(2 * lst + 1);
#pragma unroll
  for (int p = 0; p < NP; ++p) { bb[2 + p] = baddr(8 + p0 + p); bb[2 + NP + p] = baddr(14 + p0 + p); }
  u4 fa[2][NA];
  u2 fb[2][NC][2];
  unsigned a0, g0, bc[NC];
  auto bases = [&](int odd, unsigned sb, unsigned sa) {
    a0 = (odd ? abase1 : abase0) + sa; g0 = a0 + gt0 * 2048;
#pragma unroll
    for (int ci = 0; ci < NC; ++ci) bc[ci] = bb[ci] + sb;
  };
  auto memop = [&](int nb, int k, int npc, unsigned sb, unsigned sa, bool live, bool liveA) {
    if (k < 2 * NC) {
      RDTR(fb[nb][k >> 1][k & 1], bc[k >> 1], (k & 1) * 2560);
    } else if (k < 2 * NC + NA) {
      const int r = k - 2 * NC;
      if (r < NRT) RD128(fa[nb][r], a0, (RT0 + r) * 2048);
      else RD128(fa[nb][r], g0, (r - NRT) * 2048);
    } else if (k < 2 * NC + NA + npc) {
      const int i = k - 2 * NC - NA;
      k1_piece(c, lds, i, sb, sa, live, npc == 3);
    }
  };
  auto wait_frags = [&](int buf) {
    asm volatile("s_waitcnt lgkmcnt(0)"
                 : "+v"(fa[buf][0]), "+v"(fa[buf][1]), "+v"(fa[buf][2]), "+v"(fa[buf][3]),
                   "+v"(fb[buf][0][0]), "+v"(fb[buf][0][1]), "+v"(fb[buf][1][0]), "+v"(fb[buf][1][1]), "+v"(fb[buf][2][0]),
                   "+v"(fb[buf][2][1]), "+v"(fb[buf][3][0]), "+v"(fb[buf][3][1])
                 :: "memory");
#pragma unroll
    for (int r = 4; r < NA; ++r) asm volatile("" : "+v"(fa[buf][r]) :: "memory");
#pragma unroll
    for (int q = 4; q < NC; ++q) asm volatile("" : "+v"(fb[buf][q][0]), "+v"(fb[buf][q][1]) :: "memory");
  };
  WAITV(8);
  __builtin_amdgcn_s_barrier();
  STAMP(1);
  bases(0, 0, 0);
#pragma unroll
  for (int k = 0; k < 2 * NC + NA; ++k) memop(0, k, 0, 0, 0, true, true);
  unsigned sw = 0;
  unsigned sa = 0;
  auto step = [&](int b, bool first, bool live, unsigned san) {
    const unsigned sn = (sw == 4 * BSLOT) ? 0u : sw + BSLOT;
    bases(b ^ 1, sn, b ? san : sa);
    if (first) WAITV(5); else if (b == 0) WAITV(13); else WAITV(11);
    wait_frags(b);
    __builtin_amdgcn_s_barrier();
    const int npc = b ? 5 : 3;
    auto mo = [&](int k) {
      if (first && k >= 2 * NC + NA) {
        const int i = k - 2 * NC - NA;
        if (i < 3) k1_piece(c, lds, i, 3 * BSLOT, 0, true, true);
        else if (i < 8) k1_piece(c, lds, i - 3, 4 * BSLOT, 2 * ASLOT, true, false);
        else if (i < 11) k1_piece(c, lds, i - 8, 0, 0, true, true);
      } else memop(b ^ 1, k, npc, sw, sa, live, live);
    };
    int k = 0;
#pragma unroll
    for (int ci = 0; ci < NC; ++ci)
#pragma unroll
      for (int r = 0; r < NRT; ++r) {
        acc[r][ci] = MFMA(cat2(fb[b][ci][0], fb[b][ci][1]), asbf(fa[b][r]), acc[r][ci]);
        mo(k); ++k;
        __builtin_amdgcn_sched_barrier(0);
      }
#pragma unroll
    for (int r = 0; r < NG; ++r) {
      gacc[r] = MFMA(asbf(fa[b][NRT + r]), asbf(fa[b][NRT + r]), gacc[r]);
      mo(k); ++k;
      __builtin_amdgcn_sched_barrier(0);
    }
#pragma unroll
    for (int kk = NC * NRT + NG; kk < 2 * NC + NA + 11; ++kk) mo(kk);
    sw = sn;
  };
  step(0, true, true, ASLOT);
  step(1, false, true, ASLOT);
  sa = ASLOT;
#pragma unroll 1
  for (int s = 2; s < 20; s += 2) {
    const unsigned san = (sa == 2 * ASLOT) ? 0u : sa + ASLOT;
    step(0, false, s < 15, san);
    step(1, false, s + 1 < 15, san);
    sa = san;
  }
  asm volatile("s_waitcnt vmcnt(0) lgkmcnt(0)" ::: "memory");
  __builtin_amdgcn_s_barrier();
  STAMP(2);
  float* st_rs = (float*)(lds + STAT_OFF);
  float* st_sq = st_rs + 144;
  float* st_cs = st_sq + 144;
  if constexpr (NG > 0) {
    const int rr = lane & 3;
    if (((lane & 15) >> 2) == (lane >> 4)) {
#pragma unroll
      for (int r = 0; r < NG; ++r)
        st_sq[16 * (gt0 + r) + (lane & 15)] = rr == 0 ? gacc[r][0] : rr == 1 ? gacc[r][1] : rr == 2 ? gacc[r][2] : gacc[r][3];
    }
    if (lst == 3 && (lane >> 4) == 2) {
#pragma unroll
      for (int r = 0; r < NRT; ++r) st_rs[16 * (RT0 + r) + (lane & 15)] = acc[r][1][0];
    }
  }
  int ctile[NC];
  ctile[0] = 2 * lst; ctile[1] = 2 * lst + 1;
#pragma unroll
  for (int p = 0; p < NP; ++p) { ctile[2 + p] = 8 + p0 + p; ctile[2 + NP + p] = 14 + p0 + p; }
  if constexpr (MH == 0) {
    if ((lane & 15) == 0) {
#pragma unroll
      for (int ci = 0; ci < NC; ++ci) *(f4*)(st_cs + 16 * ctile[ci] + 4 * (lane >> 4)) = acc[0][ci];
    }
  }
  LBAR();
  f4 cs[NC];
  bf16_t* hp[NP];
#pragma unroll
  for (int p = 0; p < NP; ++p) hp[p] = A2 + a2_off(r0 + (lane & 15), 640 + 96 * cg + 16 * (p0 + p) + 4 * (lane >> 4));
#pragma unroll
  for (int ci = 0; ci < NC; ++ci) cs[ci] = *(const f4*)(st_cs + 16 * ctile[ci] + 4 * (lane >> 4));
#pragma unroll
  for (int r = 0; r < NRT; ++r) {
    const int row = 16 * (RT0 + r) + (lane & 15);
    const float mean = st_rs[row] * (1.0f / 640.0f);
    const float var = fmaxf(st_sq[row] * (1.0f / 640.0f) - mean * mean, 0.f);
    const float nm = -bfr(mean);
    const float sd = bfr(__builtin_amdgcn_sqrtf(bfr(bfr(var) + 1e-5f)));
    const float rinv = __builtin_amdgcn_rcpf(sd);
    const f4 nm4 = {nm, nm, nm, nm}, sd4 = {sd, sd, sd, sd};
#pragma unroll
    for (int ci = 0; ci < 2; ++ci) {
      f4 y = __builtin_elementwise_fma(cs[ci], nm4, acc[r][ci]) * rinv;
      u2 o; o[0] = pk2(y[0], y[1]); o[1] = pk2(y[2], y[3]);
      *(u2*)(lds + row * IMG_STRIDE + (16 * ctile[ci] + 4 * (lane >> 4)) * 2) = o;
    }
    if (MH == 1 || r > 0) {
      const float ga = -1.4426950408889634f * rinv;
      constexpr int dummy = 0; (void)dummy;
      const int rt = RT0 + r - 1;
#pragma unroll
      for (int p = 0; p < NP; ++p) {
        f4 t = __builtin_elementwise_fma(cs[2 + p], nm4, acc[r][2 + p]) * ga;
        f4 ut = __builtin_elementwise_fma(cs[2 + NP + p], nm4, acc[r][2 + NP + p]);
        f4 d;
#pragma unroll
        for (int e = 0; e < 4; ++e) d[e] = __builtin_amdgcn_exp2f(t[e]);
        d = __builtin_elementwise_fma(d, sd4, sd4);
#pragma unroll
        for (int e = 0; e < 4; ++e) d[e] = __builtin_amdgcn_rcpf(d[e]);
        d = d * ut;
        u2 o; o[0] = pk2(d[0], d[1]); o[1] = pk2(d[2], d[3]);
        ST8O(hp[p] + (rt >> 2) * (68 * 4 * 512), o, (rt & 3) * 1024);
      }
    }
  }
}

DEVI float xor1(float v) { return __builtin_bit_cast(float, __builtin_amdgcn_mov_dpp(__builtin_bit_cast(int, v), 0xB1, 0xf, 0xf, true)); }

__global__ __launch_bounds__(512, 2) void k1_kernel(const bf16_t* __restrict__ x, const bf16_t* __restrict__ wqkv,
                                                    const bf16_t* __restrict__ wg, const bf16_t* __restrict__ wu,
                                                    const bf16_t* __restrict__ wo, const bf16_t* __restrict__ wd,
                                                    bf16_t* __restrict__ A2, bf16_t* __restrict__ W2f) {
  __shared__ __attribute__((aligned(16))) char lds[DUMMY_OFF + 1024];
  const int tid = threadIdx.x, lane = tid & 63;
  const int w = __builtin_amdgcn_readfirstlane(tid >> 6);
  const int bid = blockIdx.x, xcd = bid & 7, idx = bid >> 3;
  const int rb = 8 * (xcd & 1) + (idx & 7), cg = 4 * (xcd >> 1) + (idx >> 3);
  const int r0 = rb * 128;
  unsigned long long* dbgp = (unsigned long long*)(A2 + 8 * 1024 * 1024);
  STAMP(0);
  K1Ctx c;
  {
#pragma unroll
    for (int i = 0; i < 5; ++i) {
      const char* src = (const char*)g_ones; unsigned str = 0, kind = 2, dst = DUMMY_OFF;
      const int pb = w + 8 * i;
      const int pa = i == 2 ? 12 + w : w + 8 * (i - 3);
      if (i < 3 && pb < 20) {
        const unsigned ap = pb * 1024 + lane * 16;
        const unsigned k = ap / 640;
        const unsigned al = ap ^ ((((k >> 1) & 1) | (((k >> 3) & 1) << 1)) << 5);
        const unsigned ch = (al - k * 640) >> 4;
        kind = 0; dst = BOFF + pb * 1024;
        if (ch < 15) {
          src = (const char*)(wqkv + k * 1920 + ((ch * 13) >> 6) * 600 + 40 * cg + 8 * ch); str = 32u * 1920 * 2;
        } else if (ch > 15) {
          src = (const char*)((ch < 28 ? wg + 8 * (ch - 16) : wu + 8 * (ch - 28)) + k * 1536 + 96 * cg); str = 32u * 1536 * 2;
        }
      } else if (i >= 3 || (i == 2 && w < 6)) {
        const int lr = 8 * pa + (lane >> 3);
        int grow = r0 - 16 + lr;
        grow = grow < 0 ? 0 : grow;
        kind = 1; dst = pa * 1024;
        if (lr != 0) { src = (const char*)(x + grow * 640 + (((lane & 7) ^ (lr & 7)) * 8)); str = i == 2 ? 64u : 128u; }
      }
      c.lo[i] = (unsigned)(uint64_t)src; c.hi[i] = (unsigned)((uint64_t)src >> 32); c.str[i] = str;
      c.pdst[i] = dst; c.kind[i] = kind;
    }
  }
  STAMP(5);
#pragma unroll
  for (int s = 0; s < 3; ++s) {
#pragma unroll
    for (int i = 0; i < ((s & 1) ? 3 : 5); ++i) k1_piece(c, lds, i, s * BSLOT, (s >> 1) * ASLOT, true, (s & 1) != 0);
    if (s < 2) __builtin_amdgcn_s_barrier();
  }
  STAMP(6);
  {
    const int lst = w < 4 ? w : ((w + 2) & 3);
    if (w < 2) k1_main<0, 2>(c, lds, lane, lst, r0, cg, A2, dbgp);
    else if (w < 4) k1_main<0, 1>(c, lds, lane, lst, r0, cg, A2, dbgp);
    else if (w < 6) k1_main<1, 1>(c, lds, lane, lst, r0, cg, A2, dbgp);
    else k1_main<1, 2>(c, lds, lane, lst, r0, cg, A2, dbgp);
  }
  const int gw = bid * 8 + w;
  unsigned short wv[2][8];
#pragma unroll
  for (int t = 0; t < 2; ++t) {
    const int fi = gw + 2048 * t;
    if (t == 0 || fi < 2720) {
      const int ks = fi / 40, ct = fi - ks * 40;
      const bf16_t* p = (ks < 20 ? wo + ks * 32 * 640 : wd + (ks - 20) * 32 * 640) + (8 * (lane >> 4)) * 640 + 16 * ct + (lane & 15);
#pragma unroll
      for (int e = 0; e < 8; ++e) wv[t][e] = p[e * 640];
    }
  }
  LBAR();
  STAMP(3);
  {
    const int half = tid & 1, pi = tid >> 1, hh = pi >> 7, row = pi & 127, pos = r0 + row;
    const char* pr = lds + (16 + row) * IMG_STRIDE + 40 * hh;
    const char* pw = pr - 8 * half * IMG_STRIDE;
    unsigned qv[10];
#pragma unroll
    for (int i = 0; i < 5; ++i) { u2 t = *(const u2*)(pr + 8 * i); qv[2 * i] = t[0]; qv[2 * i + 1] = t[1]; }
    float lg[4];
#pragma unroll
    for (int ww = 0; ww < 4; ++ww) {
      const char* pk = pw - 2 * ww * IMG_STRIDE + 80;
      float a = 0.f;
#pragma unroll
      for (int i = 0; i < 5; ++i) {
        u2 t = *(const u2*)(pk + 8 * i);
        a = dot2(qv[2 * i], t[0], a); a = dot2(qv[2 * i + 1], t[1], a);
      }
      a = bfr(bfr(a) * 0.22360679774997896f);
      lg[ww] = (pos - 2 * (4 * half + ww) >= 0) ? a : -1e9f;
    }
    float mx = fmaxf(fmaxf(lg[0], lg[1]), fmaxf(lg[2], lg[3]));
    mx = fmaxf(mx, xor1(mx));
    float sum = 0.f;
#pragma unroll
    for (int ww = 0; ww < 4; ++ww) { lg[ww] = __expf(lg[ww] - mx); sum += lg[ww]; }
    sum += xor1(sum);
    const float inv = __builtin_amdgcn_rcpf(sum);
    float o[20];
#pragma unroll
    for (int i = 0; i < 20; ++i) o[i] = 0.f;
#pragma unroll
    for (int wp = 0; wp < 2; ++wp) {
      const unsigned P = pk2(lg[2 * wp] * inv, lg[2 * wp + 1] * inv);
      const char* pv0 = pw - 4 * wp * IMG_STRIDE + 160;
      const char* pv1 = pv0 - 2 * IMG_STRIDE;
#pragma unroll
      for (int i = 0; i < 5; ++i) {
        u2 a = *(const u2*)(pv0 + 8 * i), b = *(const u2*)(pv1 + 8 * i);
#pragma unroll
        for (int e = 0; e < 2; ++e) {
          unsigned lo = __builtin_amdgcn_perm(b[e], a[e], 0x05040100u);
          unsigned hi = __builtin_amdgcn_perm(b[e], a[e], 0x07060302u);
          o[4 * i + 2 * e] = dot2(lo, P, o[4 * i + 2 * e]);
          o[4 * i + 2 * e + 1] = dot2(hi, P, o[4 * i + 2 * e + 1]);
        }
      }
    }
#pragma unroll
    for (int i = 0; i < 20; ++i) o[i] += xor1(o[i]);
    const int kc0 = 40 * cg + 20 * hh;
#pragma unroll
    for (int i = 0; i < 3; ++i) {
      const int pc = half ? 2 + i : i;
      u2 t;
      t[0] = half ? pk2(o[4 * (2 + i)], o[4 * (2 + i) + 1]) : pk2(o[4 * i], o[4 * i + 1]);
      t[1] = half ? pk2(o[4 * (2 + i) + 2], o[4 * (2 + i) + 3]) : pk2(o[4 * i + 2], o[4 * i + 3]);
      ST8(A2 + a2_off(pos, kc0 + 4 * pc), t);
    }
  }
#pragma unroll
  for (int t = 0; t < 2; ++t) {
    const int fi = gw + 2048 * t;
    if (t == 0 || fi < 2720) {
      u4 o;
#pragma unroll
      for (int e = 0; e < 4; ++e) o[e] = (unsigned)wv[t][2 * e] | ((unsigned)wv[t][2 * e + 1] << 16);
      ST16(W2f + ((size_t)fi * 64 + lane) * 8, o);
    }
  }
  STAMP(4);
}

constexpr int PD = 8;

__global__ __launch_bounds__(256, 1) void k2_kernel(const bf16_t* __restrict__ A2, const bf16_t* __restrict__ W2f,
                                                    const bf16_t* __restrict__ x, bf16_t* __restrict__ out) {
  __shared__ __attribute__((aligned(16))) char lds[4 * 15 * 1024];
  const int tid = threadIdx.x, lane = tid & 63;
  const int w = __builtin_amdgcn_readfirstlane(tid >> 6);
  const int bid = blockIdx.x, xcd = bid & 7, idx = bid >> 3;
  const int rb64 = 8 * (xcd & 3) + (idx & 7), cb = 4 * (xcd >> 2) + (idx >> 3);
  unsigned long long* dbgp = (unsigned long long*)((bf16_t*)A2 + 9 * 1024 * 1024);
  STAMP(0);
  f4 acc[4][5];
#pragma unroll
  for (int r = 0; r < 4; ++r)
#pragma unroll
    for (int q = 0; q < 5; ++q) acc[r][q] = f4{0.f, 0.f, 0.f, 0.f};
  const char* ap = (const char*)(A2 + ((size_t)(rb64 * 68 + w) * 256 + lane) * 8);
  const char* bp = (const char*)(W2f + ((size_t)(w * 40 + 5 * cb) * 64 + lane) * 8);
  u4 fa[PD][4], fb[PD][5];
  auto issue = [&](int i) {
    const char* a = ap + (size_t)i * 4 * 4096;
    const char* b = bp + (size_t)i * 4 * 40 * 1024;
#pragma unroll
    for (int r = 0; r < 4; ++r)
      asm volatile("global_load_dwordx4 %0, %1, off offset:%2" : "=v"(fa[i % PD][r]) : "v"(a), "n"(r * 1024));
#pragma unroll
    for (int q = 0; q < 4; ++q)
      asm volatile("global_load_dwordx4 %0, %1, off offset:%2" : "=a"(fb[i % PD][q]) : "v"(b), "n"(q * 1024));
    asm volatile("global_load_dwordx4 %0, %1, off offset:%2" : "=a"(fb[i % PD][4]) : "v"(b + 4096), "n"(0));
  };
#pragma unroll
  for (int i = 0; i < PD - 1; ++i) issue(i);
  const int grow = rb64 * 64 + 16 * w + (lane & 15), gcol = 80 * cb + 4 * (lane >> 4);
#pragma unroll
  for (int i = 0; i < 17; ++i) {
    const int b = i % PD;
    if (i + PD - 1 < 17) issue(i + PD - 1);
    constexpr int dummy = 0; (void)dummy;
    const int rem = (i + PD - 1 < 17 ? PD - 1 : 16 - i) * 9;
    asm volatile("s_waitcnt vmcnt(%9)" : "+v"(fa[b][0]), "+v"(fa[b][1]), "+v"(fa[b][2]), "+v"(fa[b][3]),
                 "+a"(fb[b][0]), "+a"(fb[b][1]), "+a"(fb[b][2]), "+a"(fb[b][3]), "+a"(fb[b][4]) : "n"(rem) : "memory");
#pragma unroll
    for (int ci = 0; ci < 5; ++ci)
#pragma unroll
      for (int r = 0; r < 4; ++r) acc[r][ci] = MFMA(asbf(fb[b][ci]), asbf(fa[b][r]), acc[r][ci]);
  }
  STAMP(1);
  u2 xv[5];
#pragma unroll
  for (int ci = 0; ci < 5; ++ci) xv[ci] = *(const u2*)(x + grow * 640 + gcol + 16 * ci);
#pragma unroll
  for (int r = 0; r < 4; ++r) {
    if (r != w) {
      const int slot = (w - r - 1) & 3;
#pragma unroll
      for (int q = 0; q < 5; ++q) *(f4*)(lds + ((r * 3 + slot) * 5 + q) * 1024 + lane * 16) = acc[r][q];
    }
  }
  LBAR();
  STAMP(2);
#pragma unroll
  for (int q = 0; q < 5; ++q) {
    f4 s = w == 0 ? acc[0][q] : w == 1 ? acc[1][q] : w == 2 ? acc[2][q] : acc[3][q];
#pragma unroll
    for (int t = 0; t < 3; ++t) s += *(const f4*)(lds + ((w * 3 + t) * 5 + q) * 1024 + lane * 16);
    u2 o;
    o[0] = pk2(s[0] + lo16(xv[q][0]), s[1] + hi16(xv[q][0]));
    o[1] = pk2(s[2] + lo16(xv[q][1]), s[3] + hi16(xv[q][1]));
    *(u2*)(out + grow * 640 + gcol + 16 * q) = o;
  }
  STAMP(3);
}

extern "C" void kernel_launch(void* const* d_in, const int* in_sizes, int n_in, void* d_out, int out_size,
                              void* d_ws, size_t ws_size, hipStream_t stream) {
  const bf16_t* x = (const bf16_t*)d_in[0];
  const bf16_t* wqkv = (const bf16_t*)d_in[1];
  const bf16_t* wo = (const bf16_t*)d_in[2];
  const bf16_t* wg = (const bf16_t*)d_in[4];
  const bf16_t* wu = (const bf16_t*)d_in[5];
  const bf16_t* wd = (const bf16_t*)d_in[6];
  bf16_t* A2 = (bf16_t*)d_ws;
  bf16_t* W2f = A2 + 6 * 1024 * 1024;
  k1_kernel<<<256, 512, 0, stream>>>(x, wqkv, wg, wu, wo, wd, A2, W2f);
  k2_kernel<<<256, 256, 0, stream>>>(A2, W2f, x, (bf16_t*)d_out);
}
```

```cpp
#include <hip/hip_runtime.h>
#include <hip/hip_bf16.h>
#include <stdint.h>

typedef short v4s __attribute__((ext_vector_type(4)));
typedef __bf16 bf8 __attribute__((ext_vector_type(8)));
typedef __bf16 bf2 __attribute__((ext_vector_type(2)));
typedef float f4 __attribute__((ext_vector_type(4)));
typedef unsigned u4 __attribute__((ext_vector_type(4)));
typedef unsigned u2 __attribute__((ext_vector_type(2)));
typedef unsigned short bf16_t;

#define LAS __attribute__((address_space(3)))
#define GAS __attribute__((address_space(1)))
#define DEVI __device__ __forceinline__

__device__ const __attribute__((aligned(16))) unsigned g_ones[4] = {0x3F803F80u, 0x3F803F80u, 0x3F803F80u, 0x3F803F80u};

DEVI float bfr(float v) { return (float)(__bf16)v; }
DEVI unsigned pk2(float a, float b) { bf2 c; c[0] = (__bf16)a; c[1] = (__bf16)b; return __builtin_bit_cast(unsigned, c); }
DEVI float lo16(unsigned v) { return __builtin_bit_cast(float, v << 16); }
DEVI float hi16(unsigned v) { return __builtin_bit_cast(float, v & 0xffff0000u); }
DEVI float dot2(unsigned a, unsigned b, float c) {
  return __builtin_amdgcn_fdot2_f32_bf16(__builtin_bit_cast(bf2, a), __builtin_bit_cast(bf2, b), c, false);
}
DEVI void glds16(const void* g, unsigned ldsoff, char* lds) {
  __builtin_amdgcn_global_load_lds((const GAS void*)g, (LAS void*)(lds + ldsoff), 16, 0, 0);
}
#define RD128(d, addr, off) asm volatile("ds_read_b128 %0, %1 offset:%2" : "=v"(d) : "v"(addr), "n"(off))
#define RDTR(d, addr, off) asm volatile("ds_read_b64_tr_b16 %0, %1 offset:%2" : "=v"(d) : "v"(addr), "n"(off))
DEVI bf8 cat2(u2 a, u2 b) { u4 r; r[0] = a[0]; r[1] = a[1]; r[2] = b[0]; r[3] = b[1]; return __builtin_bit_cast(bf8, r); }
DEVI bf8 asbf(u4 a) { return __builtin_bit_cast(bf8, a); }
#define MFMA(a, b, c) __builtin_amdgcn_mfma_f32_16x16x32_bf16(a, b, c, 0, 0, 0)
#define WAITV(n) asm volatile("s_waitcnt vmcnt(" #n ")" ::: "memory")
#define WAITL0() asm volatile("s_waitcnt lgkmcnt(0)" ::: "memory")

#define ST_SC
#define ST8(p, v) asm volatile("global_store_dwordx2 %0, %1, off sc0 sc1" :: "v"(p), "v"(v) : "memory")
#define ST8O(p, v, o) asm volatile("global_store_dwordx2 %0, %1, off offset:%2 sc0 sc1" :: "v"(p), "v"(v), "n"(o) : "memory")
#define ST16(p, v) asm volatile("global_store_dwordx4 %0, %1, off sc0 sc1" :: "v"(p), "v"(v) : "memory")
constexpr int ASLOT = 18432;
constexpr int BSLOT = 20480;
constexpr int BOFF = 3 * ASLOT;
constexpr int DUMMY_OFF = BOFF + 5 * BSLOT;
constexpr int IMG_STRIDE = 264;
constexpr int STAT_OFF = 144 * IMG_STRIDE;
#define STAMP(i)

DEVI unsigned a2_off(int grow, int kcol) {
  return (((((unsigned)grow >> 6) * 68 + ((unsigned)kcol >> 5)) * 4 + (((unsigned)grow >> 4) & 3)) * 64 +
          (((((unsigned)kcol >> 3) & 3) << 4) | ((unsigned)grow & 15))) * 8 + ((unsigned)kcol & 7);
}

struct K1Ctx {
  unsigned lo[5], hi[5];
  unsigned str[5];
  unsigned pdst[5];
  unsigned kind[5];
  const char* pf;
  unsigned pfs;
};
DEVI void k1_pf(K1Ctx& c, char* lds) {
  glds16(c.pf, DUMMY_OFF, lds);
  c.pf += c.pfs;
}

DEVI void k1_piece(K1Ctx& c, char* lds, int i, unsigned sb, unsigned sa, bool live, bool oddg) {
  const bool lv = (i == 2 && oddg) ? (live && c.kind[2] == 0) : live;
  const char* p = lv ? (const char*)(((uint64_t)c.hi[i] << 32) | c.lo[i]) : (const char*)g_ones;
  unsigned d;
  if (i < 2) d = sb + c.pdst[i];
  else if (i > 2) d = sa + c.pdst[i];
  else if (oddg) d = c.kind[2] == 0 ? sb + c.pdst[2] : (unsigned)DUMMY_OFF;
  else d = (c.kind[2] == 0 ? sb : c.kind[2] == 1 ? sa : 0u) + c.pdst[2];
  glds16(p, d, lds);
  asm("v_add_co_u32 %0, vcc, %0, %2\n\tv_addc_co_u32 %1, vcc, 0, %1, vcc" : "+v"(c.lo[i]), "+v"(c.hi[i]) : "v"(c.str[i]) : "vcc");
}
#define LBAR() do { asm volatile("s_waitcnt lgkmcnt(0)" ::: "memory"); __builtin_amdgcn_s_barrier(); asm volatile("" ::: "memory"); } while (0)

template <int MH, int NP>
DEVI void k1_main(K1Ctx& c, char* lds, int lane, int lst, int r0, int cg, bf16_t* __restrict__ A2,
                  unsigned long long* dbgp) {
  constexpr int NRT = MH == 0 ? 5 : 4;
  constexpr int RT0 = MH == 0 ? 0 : 5;
  constexpr int NC = 2 + 2 * NP;
  constexpr int NG = NP == 2 ? 0 : (MH == 0 ? 3 : 2);
  constexpr int NA = NRT + NG;
  f4 acc[NRT][NC];
  f4 gacc[NG + 1];
#pragma unroll
  for (int r = 0; r < NRT; ++r)
#pragma unroll
    for (int q = 0; q < NC; ++q) acc[r][q] = f4{0.f, 0.f, 0.f, 0.f};
#pragma unroll
  for (int r = 0; r < NG + 1; ++r) gacc[r] = f4{0.f, 0.f, 0.f, 0.f};
  const unsigned lbase = (unsigned)(uintptr_t)(LAS char*)lds;
  const int ar = lane & 15, ac = lane >> 4;
  const unsigned abase0 = lbase + ar * 128 + ((ac ^ (ar & 7)) * 16);
  const unsigned abase1 = lbase + ar * 128 + (((4 + ac) ^ (ar & 7)) * 16);
  const int gt0 = MH == 0 ? 3 * (lst - 2) : 6 + (lst - 2);
  const int g = lane >> 4, q4 = (lane & 15) >> 2, p4 = lane & 3;
  const int p0 = NP == 2 ? 2 * lst : lst + 2;
  const unsigned hx = (((q4 >> 1) & 1) | ((g & 1) << 1)) << 5;
  auto baddr = [&](int ct) { return lbase + BOFF + ((((8 * g + q4) * 640 + 32 * ct) ^ hx) + 8 * p4); };
  unsigned bb[NC];
  bb[0] = baddr(2 * lst); bb[1] = baddr(2 * lst + 1);
#pragma unroll
  for (int p = 0; p < NP; ++p) { bb[2 + p] = baddr(8 + p0 + p); bb[2 + NP + p] = baddr(14 + p0 + p); }
  u4 fa[2][NA];
  u2 fb[2][NC][2];
  unsigned a0, g0, bc[NC];
  auto bases = [&](int odd, unsigned sb, unsigned sa) {
    a0 = (odd ? abase1 : abase0) + sa; g0 = a0 + gt0 * 2048;
#pragma unroll
    for (int ci = 0; ci < NC; ++ci) bc[ci] = bb[ci] + sb;
  };
  auto memop = [&](int nb, int k, int npc, unsigned sb, unsigned sa, bool live, bool liveA) {
    if (k < 2 * NC) {
      RDTR(fb[nb][k >> 1][k & 1], bc[k >> 1], (k & 1) * 2560);
    } else if (k < 2 * NC + NA) {
      const int r = k - 2 * NC;
      if (r < NRT) RD128(fa[nb][r], a0, (RT0 + r) * 2048);
      else RD128(fa[nb][r], g0, (r - NRT) * 2048);
    } else if (k < 2 * NC + NA + npc) {
      const int i = k - 2 * NC - NA;
      k1_piece(c, lds, i, sb, sa, live, npc == 3);
    }
  };
  auto wait_frags = [&](int buf) {
    asm volatile("s_waitcnt lgkmcnt(0)"
                 : "+v"(fa[buf][0]), "+v"(fa[buf][1]), "+v"(fa[buf][2]), "+v"(fa[buf][3]),
                   "+v"(fb[buf][0][0]), "+v"(fb[buf][0][1]), "+v"(fb[buf][1][0]), "+v"(fb[buf][1][1]), "+v"(fb[buf][2][0]),
                   "+v"(fb[buf][2][1]), "+v"(fb[buf][3][0]), "+v"(fb[buf][3][1])
                 :: "memory");
#pragma unroll
    for (int r = 4; r < NA; ++r) asm volatile("" : "+v"(fa[buf][r]) :: "memory");
#pragma unroll
    for (int q = 4; q < NC; ++q) asm volatile("" : "+v"(fb[buf][q][0]), "+v"(fb[buf][q][1]) :: "memory");
  };
  WAITV(10);
  __builtin_amdgcn_s_barrier();
  STAMP(1);
  bases(0, 0, 0);
#pragma unroll
  for (int k = 0; k < 2 * NC + NA; ++k) memop(0, k, 0, 0, 0, true, true);
  unsigned sw = 0;
  unsigned sa = 0;
  auto step = [&](int b, bool first, bool live, unsigned san) {
    const unsigned sn = (sw == 4 * BSLOT) ? 0u : sw + BSLOT;
    bases(b ^ 1, sn, b ? san : sa);
    if (first) WAITV(6); else if (b == 0) WAITV(13); else WAITV(11);
    wait_frags(b);
    __builtin_amdgcn_s_barrier();
    const int npc = b ? 5 : 3;
    auto mo = [&](int k) {
      if (first && k >= 2 * NC + NA) {
        const int i = k - 2 * NC - NA;
        if (i < 3) k1_piece(c, lds, i, 3 * BSLOT, 0, true, true);
        else if (i == 3 || i == 9 || i == 13) k1_pf(c, lds);
        else if (i < 9) k1_piece(c, lds, i - 4, 4 * BSLOT, 2 * ASLOT, true, false);
        else if (i < 13) k1_piece(c, lds, i - 10, 0, 0, true, true);
      } else memop(b ^ 1, k, npc, sw, sa, live, live);
    };
    int k = 0;
#pragma unroll
    for (int ci = 0; ci < NC; ++ci)
#pragma unroll
      for (int r = 0; r < NRT; ++r) {
        acc[r][ci] = MFMA(cat2(fb[b][ci][0], fb[b][ci][1]), asbf(fa[b][r]), acc[r][ci]);
        mo(k); ++k;
        __builtin_amdgcn_sched_barrier(0);
      }
#pragma unroll
    for (int r = 0; r < NG; ++r) {
      gacc[r] = MFMA(asbf(fa[b][NRT + r]), asbf(fa[b][NRT + r]), gacc[r]);
      mo(k); ++k;
      __builtin_amdgcn_sched_barrier(0);
    }
#pragma unroll
    for (int kk = NC * NRT + NG; kk < 2 * NC + NA + 14; ++kk) mo(kk);
    sw = sn;
  };
  step(0, true, true, ASLOT);
  step(1, false, true, ASLOT);
  sa = ASLOT;
#pragma unroll 1
  for (int s = 2; s < 20; s += 2) {
    const unsigned san = (sa == 2 * ASLOT) ? 0u : sa + ASLOT;
    step(0, false, s < 15, san);
    step(1, false, s + 1 < 15, san);
    sa = san;
  }
  asm volatile("s_waitcnt vmcnt(0) lgkmcnt(0)" ::: "memory");
  __builtin_amdgcn_s_barrier();
  STAMP(2);
  float* st_rs = (float*)(lds + STAT_OFF);
  float* st_sq = st_rs + 144;
  float* st_cs = st_sq + 144;
  if constexpr (NG > 0) {
    const int rr = lane & 3;
    if (((lane & 15) >> 2) == (lane >> 4)) {
#pragma unroll
      for (int r = 0; r < NG; ++r)
        st_sq[16 * (gt0 + r) + (lane & 15)] = rr == 0 ? gacc[r][0] : rr == 1 ? gacc[r][1] : rr == 2 ? gacc[r][2] : gacc[r][3];
    }
    if (lst == 3 && (lane >> 4) == 2) {
#pragma unroll
      for (int r = 0; r < NRT; ++r) st_rs[16 * (RT0 + r) + (lane & 15)] = acc[r][1][0];
    }
  }
  int ctile[NC];
  ctile[0] = 2 * lst; ctile[1] = 2 * lst + 1;
#pragma unroll
  for (int p = 0; p < NP; ++p) { ctile[2 + p] = 8 + p0 + p; ctile[2 + NP + p] = 14 + p0 + p; }
  if constexpr (MH == 0) {
    if ((lane & 15) == 0) {
#pragma unroll
      for (int ci = 0; ci < NC; ++ci) *(f4*)(st_cs + 16 * ctile[ci] + 4 * (lane >> 4)) = acc[0][ci];
    }
  }
  LBAR();
  f4 cs[NC];
  bf16_t* hp[NP];
#pragma unroll
  for (int p = 0; p < NP; ++p) hp[p] = A2 + a2_off(r0 + (lane & 15), 640 + 96 * cg + 16 * (p0 + p) + 4 * (lane >> 4));
#pragma unroll
  for (int ci = 0; ci < NC; ++ci) cs[ci] = *(const f4*)(st_cs + 16 * ctile[ci] + 4 * (lane >> 4));
#pragma unroll
  for (int r = 0; r < NRT; ++r) {
    const int row = 16 * (RT0 + r) + (lane & 15);
    const float mean = st_rs[row] * (1.0f / 640.0f);
    const float var = fmaxf(st_sq[row] * (1.0f / 640.0f) - mean * mean, 0.f);
    const float nm = -bfr(mean);
    const float sd = bfr(__builtin_amdgcn_sqrtf(bfr(bfr(var) + 1e-5f)));
    const float rinv = __builtin_amdgcn_rcpf(sd);
    const f4 nm4 = {nm, nm, nm, nm}, sd4 = {sd, sd, sd, sd};
#pragma unroll
    for (int ci = 0; ci < 2; ++ci) {
      f4 y = __builtin_elementwise_fma(cs[ci], nm4, acc[r][ci]) * rinv;
      u2 o; o[0] = pk2(y[0], y[1]); o[1] = pk2(y[2], y[3]);
      *(u2*)(lds + row * IMG_STRIDE + (16 * ctile[ci] + 4 * (lane >> 4)) * 2) = o;
    }
    if (MH == 1 || r > 0) {
      const float ga = -1.4426950408889634f * rinv;
      constexpr int dummy = 0; (void)dummy;
      const int rt = RT0 + r - 1;
#pragma unroll
      for (int p = 0; p < NP; ++p) {
        f4 t = __builtin_elementwise_fma(cs[2 + p], nm4, acc[r][2 + p]) * ga;
        f4 ut = __builtin_elementwise_fma(cs[2 + NP + p], nm4, acc[r][2 + NP + p]);
        f4 d;
#pragma unroll
        for (int e = 0; e < 4; ++e) d[e] = __builtin_amdgcn_exp2f(t[e]);
        d = __builtin_elementwise_fma(d, sd4, sd4);
#pragma unroll
        for (int e = 0; e < 4; ++e) d[e] = __builtin_amdgcn_rcpf(d[e]);
        d = d * ut;
        u2 o; o[0] = pk2(d[0], d[1]); o[1] = pk2(d[2], d[3]);
        ST8O(hp[p] + (rt >> 2) * (68 * 4 * 512), o, (rt & 3) * 1024);
      }
    }
  }
}

DEVI float xor1(float v) { return __builtin_bit_cast(float, __builtin_amdgcn_mov_dpp(__builtin_bit_cast(int, v), 0xB1, 0xf, 0xf, true)); }

__global__ __launch_bounds__(512, 2) void k1_kernel(const bf16_t* __restrict__ x, const bf16_t* __restrict__ wqkv,
                                                    const bf16_t* __restrict__ wg, const bf16_t* __restrict__ wu,
                                                    const bf16_t* __restrict__ wo, const bf16_t* __restrict__ wd,
                                                    bf16_t* __restrict__ A2, bf16_t* __restrict__ W2f) {
  __shared__ __attribute__((aligned(16))) char lds[DUMMY_OFF + 1024];
  const int tid = threadIdx.x, lane = tid & 63;
  const int w = __builtin_amdgcn_readfirstlane(tid >> 6);
  const int bid = blockIdx.x, xcd = bid & 7, idx = bid >> 3;
  const int rb = 8 * (xcd & 1) + (idx & 7), cg = 4 * (xcd >> 1) + (idx >> 3);
  const int r0 = rb * 128;
  unsigned long long* dbgp = (unsigned long long*)(A2 + 8 * 1024 * 1024);
  STAMP(0);
  K1Ctx c;
  {
#pragma unroll
    for (int i = 0; i < 5; ++i) {
      const char* src = (const char*)g_ones; unsigned str = 0, kind = 2, dst = DUMMY_OFF;
      const int pb = w + 8 * i;
      const int pa = i == 2 ? 12 + w : w + 8 * (i - 3);
      if (i < 3 && pb < 20) {
        const unsigned ap = pb * 1024 + lane * 16;
        const unsigned k = ap / 640;
        const unsigned al = ap ^ ((((k >> 1) & 1) | (((k >> 3) & 1) << 1)) << 5);
        const unsigned ch = (al - k * 640) >> 4;
        kind = 0; dst = BOFF + pb * 1024;
        if (ch < 15) {
          src = (const char*)(wqkv + k * 1920 + ((ch * 13) >> 6) * 600 + 40 * cg + 8 * ch); str = 32u * 1920 * 2;
        } else if (ch > 15) {
          src = (const char*)((ch < 28 ? wg + 8 * (ch - 16) : wu + 8 * (ch - 28)) + k * 1536 + 96 * cg); str = 32u * 1536 * 2;
        }
      } else if (i >= 3 || (i == 2 && w < 6)) {
        const int lr = 8 * pa + (lane >> 3);
        int grow = r0 - 16 + lr;
        grow = grow < 0 ? 0 : grow;
        kind = 1; dst = pa * 1024;
        if (lr != 0) { src = (const char*)(x + grow * 640 + (((lane & 7) ^ (lr & 7)) * 8)); str = i == 2 ? 64u : 128u; }
      }
      c.lo[i] = (unsigned)(uint64_t)src; c.hi[i] = (unsigned)((uint64_t)src >> 32); c.str[i] = str;
      c.pdst[i] = dst; c.kind[i] = kind;
    }
  }
  {
    const int gw = bid * 8 + w;
    const char* pb = (const char*)g_ones; unsigned per = 0; int id = 0;
    if (gw < 400) { pb = (const char*)wqkv; per = 400; id = gw; }
    else if (gw < 720) { pb = (const char*)wg; per = 320; id = gw - 400; }
    else if (gw < 1040) { pb = (const char*)wu; per = 320; id = gw - 720; }
    else if (gw < 1466) { pb = (const char*)x; per = 426; id = gw - 1040; }
    else if (gw < 1599) { pb = (const char*)wo; per = 133; id = gw - 1466; }
    else if (gw < 1919) { pb = (const char*)wd; per = 320; id = gw - 1599; }
    c.pf = per ? pb + id * 1024 + lane * 16 : pb;
    c.pfs = per * 1024;
  }
  STAMP(5);
#pragma unroll
  for (int s = 0; s < 3; ++s) {
#pragma unroll
    for (int i = 0; i < ((s & 1) ? 3 : 5); ++i) k1_piece(c, lds, i, s * BSLOT, (s >> 1) * ASLOT, true, (s & 1) != 0);
    k1_pf(c, lds);
    if (s < 2) __builtin_amdgcn_s_barrier();
  }
  STAMP(6);
  {
    const int lst = w < 4 ? w : ((w + 2) & 3);
    if (w < 2) k1_main<0, 2>(c, lds, lane, lst, r0, cg, A2, dbgp);
    else if (w < 4) k1_main<0, 1>(c, lds, lane, lst, r0, cg, A2, dbgp);
    else if (w < 6) k1_main<1, 1>(c, lds, lane, lst, r0, cg, A2, dbgp);
    else k1_main<1, 2>(c, lds, lane, lst, r0, cg, A2, dbgp);
  }
  const int gw = bid * 8 + w;
  unsigned short wv[2][8];
#pragma unroll
  for (int t = 0; t < 2; ++t) {
    const int fi = gw + 2048 * t;
    if (t == 0 || fi < 2720) {
      const int ks = fi / 40, ct = fi - ks * 40;
      const bf16_t* p = (ks < 20 ? wo + ks * 32 * 640 : wd + (ks - 20) * 32 * 640) + (8 * (lane >> 4)) * 640 + 16 * ct + (lane & 15);
#pragma unroll
      for (int e = 0; e < 8; ++e) wv[t][e] = p[e * 640];
    }
  }
  LBAR();
  STAMP(3);
  {
    const int half = tid & 1, pi = tid >> 1, hh = pi >> 7, row = pi & 127, pos = r0 + row;
    const char* pr = lds + (16 + row) * IMG_STRIDE + 40 * hh;
    const char* pw = pr - 8 * half * IMG_STRIDE;
    unsigned qv[10];
#pragma unroll
    for (int i = 0; i < 5; ++i) { u2 t = *(const u2*)(pr + 8 * i); qv[2 * i] = t[0]; qv[2 * i + 1] = t[1]; }
    float lg[4];
#pragma unroll
    for (int ww = 0; ww < 4; ++ww) {
      const char* pk = pw - 2 * ww * IMG_STRIDE + 80;
      float a = 0.f;
#pragma unroll
      for (int i = 0; i < 5; ++i) {
        u2 t = *(const u2*)(pk + 8 * i);
        a = dot2(qv[2 * i], t[0], a); a = dot2(qv[2 * i + 1], t[1], a);
      }
      a = bfr(bfr(a) * 0.22360679774997896f);
      lg[ww] = (pos - 2 * (4 * half + ww) >= 0) ? a : -1e9f;
    }
    float mx = fmaxf(fmaxf(lg[0], lg[1]), fmaxf(lg[2], lg[3]));
    mx = fmaxf(mx, xor1(mx));
    float sum = 0.f;
#pragma unroll
    for (int ww = 0; ww < 4; ++ww) { lg[ww] = __expf(lg[ww] - mx); sum += lg[ww]; }
    sum += xor1(sum);
    const float inv = __builtin_amdgcn_rcpf(sum);
    float o[20];
#pragma unroll
    for (int i = 0; i < 20; ++i) o[i] = 0.f;
#pragma unroll
    for (int wp = 0; wp < 2; ++wp) {
      const unsigned P = pk2(lg[2 * wp] * inv, lg[2 * wp + 1] * inv);
      const char* pv0 = pw - 4 * wp * IMG_STRIDE + 160;
      const char* pv1 = pv0 - 2 * IMG_STRIDE;
#pragma unroll
      for (int i = 0; i < 5; ++i) {
        u2 a = *(const u2*)(pv0 + 8 * i), b = *(const u2*)(pv1 + 8 * i);
#pragma unroll
        for (int e = 0; e < 2; ++e) {
          unsigned lo = __builtin_amdgcn_perm(b[e], a[e], 0x05040100u);
          unsigned hi = __builtin_amdgcn_perm(b[e], a[e], 0x07060302u);
          o[4 * i + 2 * e] = dot2(lo, P, o[4 * i + 2 * e]);
          o[4 * i + 2 * e + 1] = dot2(hi, P, o[4 * i + 2 * e + 1]);
        }
      }
    }
#pragma unroll
    for (int i = 0; i < 20; ++i) o[i] += xor1(o[i]);
    const int kc0 = 40 * cg + 20 * hh;
#pragma unroll
    for (int i = 0; i < 3; ++i) {
      const int pc = half ? 2 + i : i;
      u2 t;
      t[0] = half ? pk2(o[4 * (2 + i)], o[4 * (2 + i) + 1]) : pk2(o[4 * i], o[4 * i + 1]);
      t[1] = half ? pk2(o[4 * (2 + i) + 2], o[4 * (2 + i) + 3]) : pk2(o[4 * i + 2], o[4 * i + 3]);
      ST8(A2 + a2_off(pos, kc0 + 4 * pc), t);
    }
  }
#pragma unroll
  for (int t = 0; t < 2; ++t) {
    const int fi = gw + 2048 * t;
    if (t == 0 || fi < 2720) {
      u4 o;
#pragma unroll
      for (int e = 0; e < 4; ++e) o[e] = (unsigned)wv[t][2 * e] | ((unsigned)wv[t][2 * e + 1] << 16);
      ST16(W2f + ((size_t)fi * 64 + lane) * 8, o);
    }
  }
  STAMP(4);
}

constexpr int PD = 8;

__global__ __launch_bounds__(256, 1) void k2_kernel(const bf16_t* __restrict__ A2, const bf16_t* __restrict__ W2f,
                                                    const bf16_t* __restrict__ x, bf16_t* __restrict__ out) {
  __shared__ __attribute__((aligned(16))) char lds[4 * 15 * 1024];
  const int tid = threadIdx.x, lane = tid & 63;
  const int w = __builtin_amdgcn_readfirstlane(tid >> 6);
  const int bid = blockIdx.x, xcd = bid & 7, idx = bid >> 3;
  const int rb64 = 8 * (xcd & 3) + (idx & 7), cb = 4 * (xcd >> 2) + (idx >> 3);
  unsigned long long* dbgp = (unsigned long long*)((bf16_t*)A2 + 9 * 1024 * 1024);
  STAMP(0);
  f4 acc[4][5];
#pragma unroll
  for (int r = 0; r < 4; ++r)
#pragma unroll
    for (int q = 0; q < 5; ++q) acc[r][q] = f4{0.f, 0.f, 0.f, 0.f};
  const char* ap = (const char*)(A2 + ((size_t)(rb64 * 68 + w) * 256 + lane) * 8);
  const char* bp = (const char*)(W2f + ((size_t)(w * 40 + 5 * cb) * 64 + lane) * 8);
  u4 fa[PD][4], fb[PD][5];
  auto issue = [&](int i) {
    const char* a = ap + (size_t)i * 4 * 4096;
    const char* b = bp + (size_t)i * 4 * 40 * 1024;
#pragma unroll
    for (int r = 0; r < 4; ++r)
      asm volatile("global_load_dwordx4 %0, %1, off offset:%2" : "=v"(fa[i % PD][r]) : "v"(a), "n"(r * 1024));
#pragma unroll
    for (int q = 0; q < 4; ++q)
      asm volatile("global_load_dwordx4 %0, %1, off offset:%2" : "=a"(fb[i % PD][q]) : "v"(b), "n"(q * 1024));
    asm volatile("global_load_dwordx4 %0, %1, off offset:%2" : "=a"(fb[i % PD][4]) : "v"(b + 4096), "n"(0));
  };
  const int grow = rb64 * 64 + 16 * w + (lane & 15), gcol = 80 * cb + 4 * (lane >> 4);
  u2 xv[5];
#pragma unroll
  for (int i = 0; i < PD - 1; ++i) issue(i);
#pragma unroll
  for (int i = 0; i < 17; ++i) {
    const int b = i % PD;
    if (i + PD - 1 < 17) issue(i + PD - 1);
    if (i + PD - 1 == 17) {
      const bf16_t* xp = x + grow * 640 + gcol;
#pragma unroll
      for (int ci = 0; ci < 5; ++ci)
        asm volatile("global_load_dwordx2 %0, %1, off offset:%2" : "=v"(xv[ci]) : "v"(xp), "n"(ci * 32));
    }
    const int rem = i + PD - 1 < 17 ? (PD - 1) * 9 : (16 - i) * 9 + 5;
    asm volatile("s_waitcnt vmcnt(%9)" : "+v"(fa[b][0]), "+v"(fa[b][1]), "+v"(fa[b][2]), "+v"(fa[b][3]),
                 "+a"(fb[b][0]), "+a"(fb[b][1]), "+a"(fb[b][2]), "+a"(fb[b][3]), "+a"(fb[b][4]) : "n"(rem) : "memory");
#pragma unroll
    for (int ci = 0; ci < 5; ++ci)
#pragma unroll
      for (int r = 0; r < 4; ++r) acc[r][ci] = MFMA(asbf(fb[b][ci]), asbf(fa[b][r]), acc[r][ci]);
  }
  STAMP(1);
#pragma unroll
  for (int r = 0; r < 4; ++r) {
    if (r != w) {
      const int slot = (w - r - 1) & 3;
#pragma unroll
      for (int q = 0; q < 5; ++q) *(f4*)(lds + ((r * 3 + slot) * 5 + q) * 1024 + lane * 16) = acc[r][q];
    }
  }
  LBAR();
  asm volatile("s_waitcnt vmcnt(0)" : "+v"(xv[0]), "+v"(xv[1]), "+v"(xv[2]), "+v"(xv[3]), "+v"(xv[4]) :: "memory");
  STAMP(2);
#pragma unroll
  for (int q = 0; q < 5; ++q) {
    f4 s = w == 0 ? acc[0][q] : w == 1 ? acc[1][q] : w == 2 ? acc[2][q] : acc[3][q];
#pragma unroll
    for (int t = 0; t < 3; ++t) s += *(const f4*)(lds + ((w * 3 + t) * 5 + q) * 1024 + lane * 16);
    u2 o;
    o[0] = pk2(s[0] + lo16(xv[q][0]), s[1] + hi16(xv[q][0]));
    o[1] = pk2(s[2] + lo16(xv[q][1]), s[3] + hi16(xv[q][1]));
    *(u2*)(out + grow * 640 + gcol + 16 * q) = o;
  }
  STAMP(3);
}

extern "C" void kernel_launch(void* const* d_in, const int* in_sizes, int n_in, void* d_out, int out_size,
                              void* d_ws, size_t ws_size, hipStream_t stream) {
  const bf16_t* x = (const bf16_t*)d_in[0];
  const bf16_t* wqkv = (const bf16_t*)d_in[1];
  const bf16_t* wo = (const bf16_t*)d_in[2];
  const bf16_t* wg = (const bf16_t*)d_in[4];
  const bf16_t* wu = (const bf16_t*)d_in[5];
  const bf16_t* wd = (const bf16_t*)d_in[6];
  bf16_t* A2 = (bf16_t*)d_ws;
  bf16_t* W2f = A2 + 6 * 1024 * 1024;
  k1_kernel<<<256, 512, 0, stream>>>(x, wqkv, wg, wu, wo, wd, A2, W2f);
  k2_kernel<<<256, 256, 0, stream>>>(A2, W2f, x, (bf16_t*)d_out);
}
```

```cpp
#include <hip/hip_runtime.h>
#include <hip/hip_bf16.h>
#include <stdint.h>

typedef short v4s __attribute__((ext_vector_type(4)));
typedef __bf16 bf8 __attribute__((ext_vector_type(8)));
typedef __bf16 bf2 __attribute__((ext_vector_type(2)));
typedef float f4 __attribute__((ext_vector_type(4)));
typedef unsigned u4 __attribute__((ext_vector_type(4)));
typedef unsigned u2 __attribute__((ext_vector_type(2)));
typedef unsigned short bf16_t;

#define LAS __attribute__((address_space(3)))
#define GAS __attribute__((address_space(1)))
#define DEVI __device__ __forceinline__

__device__ const __attribute__((aligned(16))) unsigned g_ones[4] = {0x3F803F80u, 0x3F803F80u, 0x3F803F80u, 0x3F803F80u};

DEVI float bfr(float v) { return (float)(__bf16)v; }
DEVI unsigned pk2(float a, float b) { bf2 c; c[0] = (__bf16)a; c[1] = (__bf16)b; return __builtin_bit_cast(unsigned, c); }
DEVI float lo16(unsigned v) { return __builtin_bit_cast(float, v << 16); }
DEVI float hi16(unsigned v) { return __builtin_bit_cast(float, v & 0xffff0000u); }
DEVI float dot2(unsigned a, unsigned b, float c) {
  return __builtin_amdgcn_fdot2_f32_bf16(__builtin_bit_cast(bf2, a), __builtin_bit_cast(bf2, b), c, false);
}
DEVI void glds16(const void* g, unsigned ldsoff, char* lds) {
  __builtin_amdgcn_global_load_lds((const GAS void*)g, (LAS void*)(lds + ldsoff), 16, 0, 0);
}
#define RD128(d, addr, off) asm volatile("ds_read_b128 %0, %1 offset:%2" : "=v"(d) : "v"(addr), "n"(off))
#define RDTR(d, addr, off) asm volatile("ds_read_b64_tr_b16 %0, %1 offset:%2" : "=v"(d) : "v"(addr), "n"(off))
DEVI bf8 cat2(u2 a, u2 b) { u4 r; r[0] = a[0]; r[1] = a[1]; r[2] = b[0]; r[3] = b[1]; return __builtin_bit_cast(bf8, r); }
DEVI bf8 asbf(u4 a) { return __builtin_bit_cast(bf8, a); }
#define MFMA(a, b, c) __builtin_amdgcn_mfma_f32_16x16x32_bf16(a, b, c, 0, 0, 0)
#define WAITV(n) asm volatile("s_waitcnt vmcnt(" #n ")" ::: "memory")
#define WAITL0() asm volatile("s_waitcnt lgkmcnt(0)" ::: "memory")

#define ST_SC
#define ST8(p, v) asm volatile("global_store_dwordx2 %0, %1, off sc0 sc1" :: "v"(p), "v"(v) : "memory")
#define ST8O(p, v, o) asm volatile("global_store_dwordx2 %0, %1, off offset:%2 sc0 sc1" :: "v"(p), "v"(v), "n"(o) : "memory")
#define ST16(p, v) asm volatile("global_store_dwordx4 %0, %1, off sc0 sc1\n\ts_nop 1" :: "v"(p), "v"(v) : "memory")
#define GM 4
constexpr int ASLOT = 18432;
constexpr int BSLOT = 20480;
constexpr int BOFF = 3 * ASLOT;
constexpr int DUMMY_OFF = BOFF + 5 * BSLOT;
constexpr int IMG_STRIDE = 264;
constexpr int STAT_OFF = 144 * IMG_STRIDE;
#define STAMP(i)

DEVI unsigned a2_off(int grow, int kcol) {
  return (((((unsigned)grow >> 6) * 68 + ((unsigned)kcol >> 5)) * 4 + (((unsigned)grow >> 4) & 3)) * 64 +
          (((((unsigned)kcol >> 3) & 3) << 4) | ((unsigned)grow & 15))) * 8 + ((unsigned)kcol & 7);
}

struct K1Ctx {
  unsigned lo[5], hi[5];
  unsigned str[5];
  unsigned pdst[5];
  unsigned kind[5];
  const char* pf;
  unsigned pfs;
};
DEVI void k1_pf(K1Ctx& c, char* lds) {
  glds16(c.pf, DUMMY_OFF, lds);
  c.pf += c.pfs;
}

DEVI void k1_piece(K1Ctx& c, char* lds, int i, unsigned sb, unsigned sa, bool live, bool oddg) {
  const bool lv = (i == 2 && oddg) ? (live && c.kind[2] == 0) : live;
  const char* p = lv ? (const char*)(((uint64_t)c.hi[i] << 32) | c.lo[i]) : (const char*)g_ones;
  unsigned d;
  if (i < 2) d = sb + c.pdst[i];
  else if (i > 2) d = sa + c.pdst[i];
  else if (oddg) d = c.kind[2] == 0 ? sb + c.pdst[2] : (unsigned)DUMMY_OFF;
  else d = (c.kind[2] == 0 ? sb : c.kind[2] == 1 ? sa : 0u) + c.pdst[2];
  glds16(p, d, lds);
  asm("v_add_co_u32 %0, vcc, %0, %2\n\tv_addc_co_u32 %1, vcc, 0, %1, vcc" : "+v"(c.lo[i]), "+v"(c.hi[i]) : "v"(c.str[i]) : "vcc");
}
#define LBAR() do { asm volatile("s_waitcnt lgkmcnt(0)" ::: "memory"); __builtin_amdgcn_s_barrier(); asm volatile("" ::: "memory"); } while (0)

template <int MH, int NP>
DEVI void k1_main(K1Ctx& c, char* lds, int lane, int lst, int r0, int cg, bf16_t* __restrict__ A2,
                  unsigned long long* dbgp) {
  constexpr int NRT = MH == 0 ? 5 : 4;
  constexpr int RT0 = MH == 0 ? 0 : 5;
  constexpr int NC = 2 + 2 * NP;
  constexpr int NG = NP == 2 ? 0 : (MH == 0 ? 3 : 2);
  constexpr int NA = NRT + NG;
  f4 acc[NRT][NC];
  f4 gacc[NG + 1];
#pragma unroll
  for (int r = 0; r < NRT; ++r)
#pragma unroll
    for (int q = 0; q < NC; ++q) acc[r][q] = f4{0.f, 0.f, 0.f, 0.f};
#pragma unroll
  for (int r = 0; r < NG + 1; ++r) gacc[r] = f4{0.f, 0.f, 0.f, 0.f};
  const unsigned lbase = (unsigned)(uintptr_t)(LAS char*)lds;
  const int ar = lane & 15, ac = lane >> 4;
  const unsigned abase0 = lbase + ar * 128 + ((ac ^ (ar & 7)) * 16);
  const unsigned abase1 = lbase + ar * 128 + (((4 + ac) ^ (ar & 7)) * 16);
  const int gt0 = MH == 0 ? 3 * (lst - 2) : 6 + (lst - 2);
  const int g = lane >> 4, q4 = (lane & 15) >> 2, p4 = lane & 3;
  const int p0 = NP == 2 ? 2 * lst : lst + 2;
  const unsigned hx = (((q4 >> 1) & 1) | ((g & 1) << 1)) << 5;
  auto baddr = [&](int ct) { return lbase + BOFF + ((((8 * g + q4) * 640 + 32 * ct) ^ hx) + 8 * p4); };
  unsigned bb[NC];
  bb[0] = baddr(2 * lst); bb[1] = baddr(2 * lst + 1);
#pragma unroll
  for (int p = 0; p < NP; ++p) { bb[2 + p] = baddr(8 + p0 + p); bb[2 + NP + p] = baddr(14 + p0 + p); }
  u4 fa[2][NA];
  u2 fb[2][NC][2];
  unsigned a0, g0, bc[NC];
  auto bases = [&](int odd, unsigned sb, unsigned sa) {
    a0 = (odd ? abase1 : abase0) + sa; g0 = a0 + gt0 * 2048;
#pragma unroll
    for (int ci = 0; ci < NC; ++ci) bc[ci] = bb[ci] + sb;
  };
  auto memop = [&](int nb, int k, int npc, unsigned sb, unsigned sa, bool live, bool liveA) {
    if (k < 2 * NC) {
      RDTR(fb[nb][k >> 1][k & 1], bc[k >> 1], (k & 1) * 2560);
    } else if (k < 2 * NC + NA) {
      const int r = k - 2 * NC;
      if (r < NRT) RD128(fa[nb][r], a0, (RT0 + r) * 2048);
      else RD128(fa[nb][r], g0, (r - NRT) * 2048);
    } else if (k < 2 * NC + NA + npc) {
      const int i = k - 2 * NC - NA;
      k1_piece(c, lds, i, sb, sa, live, npc == 3);
    }
  };
  auto wait_frags = [&](int buf) {
    asm volatile("s_waitcnt lgkmcnt(0)"
                 : "+v"(fa[buf][0]), "+v"(fa[buf][1]), "+v"(fa[buf][2]), "+v"(fa[buf][3]),
                   "+v"(fb[buf][0][0]), "+v"(fb[buf][0][1]), "+v"(fb[buf][1][0]), "+v"(fb[buf][1][1]), "+v"(fb[buf][2][0]),
                   "+v"(fb[buf][2][1]), "+v"(fb[buf][3][0]), "+v"(fb[buf][3][1])
                 :: "memory");
#pragma unroll
    for (int r = 4; r < NA; ++r) asm volatile("" : "+v"(fa[buf][r]) :: "memory");
#pragma unroll
    for (int q = 4; q < NC; ++q) asm volatile("" : "+v"(fb[buf][q][0]), "+v"(fb[buf][q][1]) :: "memory");
  };
  WAITV(10);
  __builtin_amdgcn_s_barrier();
  STAMP(1);
  bases(0, 0, 0);
#pragma unroll
  for (int k = 0; k < 2 * NC + NA; ++k) memop(0, k, 0, 0, 0, true, true);
  unsigned sw = 0;
  unsigned sa = 0;
  auto step = [&](int b, bool first, bool live, unsigned san, bool second = false) {
    const unsigned sn = (sw == 4 * BSLOT) ? 0u : sw + BSLOT;
    bases(b ^ 1, sn, b ? san : sa);
    if (first) WAITV(6); else if (b == 0) WAITV(13); else if (second) WAITV(14); else WAITV(11);
    wait_frags(b);
    __builtin_amdgcn_s_barrier();
    const int npc = b ? 5 : 3;
    auto mo = [&](int k) {
      if (first && k >= 2 * NC + NA) {
        const int i = k - 2 * NC - NA;
        if (i < 3) k1_piece(c, lds, i, 3 * BSLOT, 0, true, true);
        else if (i == 3 || i == 9 || i == 13) k1_pf(c, lds);
        else if (i < 9) k1_piece(c, lds, i - 4, 4 * BSLOT, 2 * ASLOT, true, false);
        else if (i < 13) k1_piece(c, lds, i - 10, 0, 0, true, true);
      } else {
        if (first) memop(b ^ 1, k, npc, sw, sa, live, live);
        else if ((k % GM) == GM - 1 && (k / GM) < npc) memop(b ^ 1, 2 * NC + NA + (k / GM), npc, sw, sa, live, live);
        else { const int kr = k - ((k / GM) < npc ? (k / GM) : npc); if (kr < 2 * NC + NA) memop(b ^ 1, kr, npc, sw, sa, live, live); }
      }
    };
    int k = 0;
#pragma unroll
    for (int ci = 0; ci < NC; ++ci)
#pragma unroll
      for (int r = 0; r < NRT; ++r) {
        acc[r][ci] = MFMA(cat2(fb[b][ci][0], fb[b][ci][1]), asbf(fa[b][r]), acc[r][ci]);
        mo(k); ++k;
        __builtin_amdgcn_sched_barrier(0);
      }
#pragma unroll
    for (int r = 0; r < NG; ++r) {
      gacc[r] = MFMA(asbf(fa[b][NRT + r]), asbf(fa[b][NRT + r]), gacc[r]);
      mo(k); ++k;
      __builtin_amdgcn_sched_barrier(0);
    }
#pragma unroll
    for (int kk = NC * NRT + NG; kk < 2 * NC + NA + 14; ++kk) mo(kk);
    sw = sn;
  };
  step(0, true, true, ASLOT);
  step(1, false, true, ASLOT, true);
  sa = ASLOT;
#pragma unroll 1
  for (int s = 2; s < 20; s += 2) {
    const unsigned san = (sa == 2 * ASLOT) ? 0u : sa + ASLOT;
    step(0, false, s < 15, san);
    step(1, false, s + 1 < 15, san);
    sa = san;
  }
  asm volatile("s_waitcnt vmcnt(0) lgkmcnt(0)" ::: "memory");
  __builtin_amdgcn_s_barrier();
  STAMP(2);
  float* st_rs = (float*)(lds + STAT_OFF);
  float* st_sq = st_rs + 144;
  float* st_cs = st_sq + 144;
  if constexpr (NG > 0) {
    const int rr = lane & 3;
    if (((lane & 15) >> 2) == (lane >> 4)) {
#pragma unroll
      for (int r = 0; r < NG; ++r)
        st_sq[16 * (gt0 + r) + (lane & 15)] = rr == 0 ? gacc[r][0] : rr == 1 ? gacc[r][1] : rr == 2 ? gacc[r][2] : gacc[r][3];
    }
    if (lst == 3 && (lane >> 4) == 2) {
#pragma unroll
      for (int r = 0; r < NRT; ++r) st_rs[16 * (RT0 + r) + (lane & 15)] = acc[r][1][0];
    }
  }
  int ctile[NC];
  ctile[0] = 2 * lst; ctile[1] = 2 * lst + 1;
#pragma unroll
  for (int p = 0; p < NP; ++p) { ctile[2 + p] = 8 + p0 + p; ctile[2 + NP + p] = 14 + p0 + p; }
  if constexpr (MH == 0) {
    if ((lane & 15) == 0) {
#pragma unroll
      for (int ci = 0; ci < NC; ++ci) *(f4*)(st_cs + 16 * ctile[ci] + 4 * (lane >> 4)) = acc[0][ci];
    }
  }
  LBAR();
  f4 cs[NC];
  bf16_t* hp[NP];
#pragma unroll
  for (int p = 0; p < NP; ++p) hp[p] = A2 + a2_off(r0 + (lane & 15), 640 + 96 * cg + 16 * (p0 + p) + 4 * (lane >> 4));
#pragma unroll
  for (int ci = 0; ci < NC; ++ci) cs[ci] = *(const f4*)(st_cs + 16 * ctile[ci] + 4 * (lane >> 4));
#pragma unroll
  for (int r = 0; r < NRT; ++r) {
    const int row = 16 * (RT0 + r) + (lane & 15);
    const float mean = st_rs[row] * (1.0f / 640.0f);
    const float var = fmaxf(st_sq[row] * (1.0f / 640.0f) - mean * mean, 0.f);
    const float nm = -bfr(mean);
    const float sd = bfr(__builtin_amdgcn_sqrtf(bfr(bfr(var) + 1e-5f)));
    const float rinv = __builtin_amdgcn_rcpf(sd);
    const f4 nm4 = {nm, nm, nm, nm}, sd4 = {sd, sd, sd, sd};
#pragma unroll
    for (int ci = 0; ci < 2; ++ci) {
      f4 y = __builtin_elementwise_fma(cs[ci], nm4, acc[r][ci]) * rinv;
      u2 o; o[0] = pk2(y[0], y[1]); o[1] = pk2(y[2], y[3]);
      *(u2*)(lds + row * IMG_STRIDE + (16 * ctile[ci] + 4 * (lane >> 4)) * 2) = o;
    }
    if (MH == 1 || r > 0) {
      const float ga = -1.4426950408889634f * rinv;
      constexpr int dummy = 0; (void)dummy;
      const int rt = RT0 + r - 1;
#pragma unroll
      for (int p = 0; p < NP; ++p) {
        f4 t = __builtin_elementwise_fma(cs[2 + p], nm4, acc[r][2 + p]) * ga;
        f4 ut = __builtin_elementwise_fma(cs[2 + NP + p], nm4, acc[r][2 + NP + p]);
        f4 d;
#pragma unroll
        for (int e = 0; e < 4; ++e) d[e] = __builtin_amdgcn_exp2f(t[e]);
        d = __builtin_elementwise_fma(d, sd4, sd4);
#pragma unroll
        for (int e = 0; e < 4; ++e) d[e] = __builtin_amdgcn_rcpf(d[e]);
        d = d * ut;
        u2 o; o[0] = pk2(d[0], d[1]); o[1] = pk2(d[2], d[3]);
        ST8O(hp[p] + (rt >> 2) * (68 * 4 * 512), o, (rt & 3) * 1024);
      }
    }
  }
}

DEVI float xor1(float v) { return __builtin_bit_cast(float, __builtin_amdgcn_mov_dpp(__builtin_bit_cast(int, v), 0xB1, 0xf, 0xf, true)); }

__global__ __launch_bounds__(512, 2) void k1_kernel(const bf16_t* __restrict__ x, const bf16_t* __restrict__ wqkv,
                                                    const bf16_t* __restrict__ wg, const bf16_t* __restrict__ wu,
                                                    const bf16_t* __restrict__ wo, const bf16_t* __restrict__ wd,
                                                    bf16_t* __restrict__ A2, bf16_t* __restrict__ W2f) {
  __shared__ __attribute__((aligned(16))) char lds[DUMMY_OFF + 1024];
  const int tid = threadIdx.x, lane = tid & 63;
  const int w = __builtin_amdgcn_readfirstlane(tid >> 6);
  const int bid = blockIdx.x, xcd = bid & 7, idx = bid >> 3;
  const int rb = 8 * (xcd & 1) + (idx & 7), cg = 4 * (xcd >> 1) + (idx >> 3);
  const int r0 = rb * 128;
  unsigned long long* dbgp = (unsigned long long*)(A2 + 8 * 1024 * 1024);
  STAMP(0);
  K1Ctx c;
  {
#pragma unroll
    for (int i = 0; i < 5; ++i) {
      const char* src = (const char*)g_ones; unsigned str = 0, kind = 2, dst = DUMMY_OFF;
      const int pb = w + 8 * i;
      const int pa = i == 2 ? 12 + w : w + 8 * (i - 3);
      if (i < 3 && pb < 20) {
        const unsigned ap = pb * 1024 + lane * 16;
        const unsigned k = ap / 640;
        const unsigned al = ap ^ ((((k >> 1) & 1) | (((k >> 3) & 1) << 1)) << 5);
        const unsigned ch = (al - k * 640) >> 4;
        kind = 0; dst = BOFF + pb * 1024;
        if (ch < 15) {
          src = (const char*)(wqkv + k * 1920 + ((ch * 13) >> 6) * 600 + 40 * cg + 8 * ch); str = 32u * 1920 * 2;
        } else if (ch > 15) {
          src = (const char*)((ch < 28 ? wg + 8 * (ch - 16) : wu + 8 * (ch - 28)) + k * 1536 + 96 * cg); str = 32u * 1536 * 2;
        }
      } else if (i >= 3 || (i == 2 && w < 6)) {
        const int lr = 8 * pa + (lane >> 3);
        int grow = r0 - 16 + lr;
        grow = grow < 0 ? 0 : grow;
        kind = 1; dst = pa * 1024;
        if (lr != 0) { src = (const char*)(x + grow * 640 + (((lane & 7) ^ (lr & 7)) * 8)); str = i == 2 ? 64u : 128u; }
      }
      c.lo[i] = (unsigned)(uint64_t)src; c.hi[i] = (unsigned)((uint64_t)src >> 32); c.str[i] = str;
      c.pdst[i] = dst; c.kind[i] = kind;
    }
  }
  {
    const int gw = bid * 8 + w;
    const char* pb = (const char*)g_ones; unsigned per = 0; int id = 0;
    if (gw < 400) { pb = (const char*)wqkv; per = 400; id = gw; }
    else if (gw < 720) { pb = (const char*)wg; per = 320; id = gw - 400; }
    else if (gw < 1040) { pb = (const char*)wu; per = 320; id = gw - 720; }
    else if (gw < 1466) { pb = (const char*)x; per = 426; id = gw - 1040; }
    else if (gw < 1599) { pb = (const char*)wo; per = 133; id = gw - 1466; }
    else if (gw < 1919) { pb = (const char*)wd; per = 320; id = gw - 1599; }
    c.pf = per ? pb + id * 1024 + lane * 16 : pb;
    c.pfs = per * 1024;
  }
  STAMP(5);
#pragma unroll
  for (int s = 0; s < 3; ++s) {
#pragma unroll
    for (int i = 0; i < ((s & 1) ? 3 : 5); ++i) k1_piece(c, lds, i, s * BSLOT, (s >> 1) * ASLOT, true, (s & 1) != 0);
    k1_pf(c, lds);
    if (s < 2) __builtin_amdgcn_s_barrier();
  }
  STAMP(6);
  {
    const int lst = w < 4 ? w : ((w + 2) & 3);
    if (w < 2) k1_main<0, 2>(c, lds, lane, lst, r0, cg, A2, dbgp);
    else if (w < 4) k1_main<0, 1>(c, lds, lane, lst, r0, cg, A2, dbgp);
    else if (w < 6) k1_main<1, 1>(c, lds, lane, lst, r0, cg, A2, dbgp);
    else k1_main<1, 2>(c, lds, lane, lst, r0, cg, A2, dbgp);
  }
  const int gw = bid * 8 + w;
  unsigned short wv[2][8];
#pragma unroll
  for (int t = 0; t < 2; ++t) {
    const int fi = gw + 2048 * t;
    if (t == 0 || fi < 2720) {
      const int ks = fi / 40, ct = fi - ks * 40;
      const bf16_t* p = (ks < 20 ? wo + ks * 32 * 640 : wd + (ks - 20) * 32 * 640) + (8 * (lane >> 4)) * 640 + 16 * ct + (lane & 15);
#pragma unroll
      for (int e = 0; e < 8; ++e) wv[t][e] = p[e * 640];
    }
  }
  LBAR();
  STAMP(3);
  {
    const int half = tid & 1, pi = tid >> 1, hh = pi >> 7, row = pi & 127, pos = r0 + row;
    const char* pr = lds + (16 + row) * IMG_STRIDE + 40 * hh;
    const char* pw = pr - 8 * half * IMG_STRIDE;
    unsigned qv[10];
#pragma unroll
    for (int i = 0; i < 5; ++i) { u2 t = *(const u2*)(pr + 8 * i); qv[2 * i] = t[0]; qv[2 * i + 1] = t[1]; }
    float lg[4];
#pragma unroll
    for (int ww = 0; ww < 4; ++ww) {
      const char* pk = pw - 2 * ww * IMG_STRIDE + 80;
      float a = 0.f;
#pragma unroll
      for (int i = 0; i < 5; ++i) {
        u2 t = *(const u2*)(pk + 8 * i);
        a = dot2(qv[2 * i], t[0], a); a = dot2(qv[2 * i + 1], t[1], a);
      }
      a = bfr(bfr(a) * 0.22360679774997896f);
      lg[ww] = (pos - 2 * (4 * half + ww) >= 0) ? a : -1e9f;
    }
    float mx = fmaxf(fmaxf(lg[0], lg[1]), fmaxf(lg[2], lg[3]));
    mx = fmaxf(mx, xor1(mx));
    float sum = 0.f;
#pragma unroll
    for (int ww = 0; ww < 4; ++ww) { lg[ww] = __expf(lg[ww] - mx); sum += lg[ww]; }
    sum += xor1(sum);
    const float inv = __builtin_amdgcn_rcpf(sum);
    float o[20];
#pragma unroll
    for (int i = 0; i < 20; ++i) o[i] = 0.f;
#pragma unroll
    for (int wp = 0; wp < 2; ++wp) {
      const unsigned P = pk2(lg[2 * wp] * inv, lg[2 * wp + 1] * inv);
      const char* pv0 = pw - 4 * wp * IMG_STRIDE + 160;
      const char* pv1 = pv0 - 2 * IMG_STRIDE;
#pragma unroll
      for (int i = 0; i < 5; ++i) {
        u2 a = *(const u2*)(pv0 + 8 * i), b = *(const u2*)(pv1 + 8 * i);
#pragma unroll
        for (int e = 0; e < 2; ++e) {
          unsigned lo = __builtin_amdgcn_perm(b[e], a[e], 0x05040100u);
          unsigned hi = __builtin_amdgcn_perm(b[e], a[e], 0x07060302u);
          o[4 * i + 2 * e] = dot2(lo, P, o[4 * i + 2 * e]);
          o[4 * i + 2 * e + 1] = dot2(hi, P, o[4 * i + 2 * e + 1]);
        }
      }
    }
#pragma unroll
    for (int i = 0; i < 20; ++i) o[i] += xor1(o[i]);
    const int kc0 = 40 * cg + 20 * hh;
#pragma unroll
    for (int i = 0; i < 3; ++i) {
      const int pc = half ? 2 + i : i;
      u2 t;
      t[0] = half ? pk2(o[4 * (2 + i)], o[4 * (2 + i) + 1]) : pk2(o[4 * i], o[4 * i + 1]);
      t[1] = half ? pk2(o[4 * (2 + i) + 2], o[4 * (2 + i) + 3]) : pk2(o[4 * i + 2], o[4 * i + 3]);
      ST8(A2 + a2_off(pos, kc0 + 4 * pc), t);
    }
  }
#pragma unroll
  for (int t = 0; t < 2; ++t) {
    const int fi = gw + 2048 * t;
    if (t == 0 || fi < 2720) {
      u4 o;
#pragma unroll
      for (int e = 0; e < 4; ++e) o[e] = (unsigned)wv[t][2 * e] | ((unsigned)wv[t][2 * e + 1] << 16);
      ST16(W2f + ((size_t)fi * 64 + lane) * 8, o);
    }
  }
  STAMP(4);
}

constexpr int PD = 8;

__global__ __launch_bounds__(256, 1) void k2_kernel(const bf16_t* __restrict__ A2, const bf16_t* __restrict__ W2f,
                                                    const bf16_t* __restrict__ x, bf16_t* __restrict__ out) {
  __shared__ __attribute__((aligned(16))) char lds[4 * 15 * 1024];
  const int tid = threadIdx.x, lane = tid & 63;
  const int w = __builtin_amdgcn_readfirstlane(tid >> 6);
  const int bid = blockIdx.x, xcd = bid & 7, idx = bid >> 3;
  const int rb64 = 8 * (xcd & 3) + (idx & 7), cb = 4 * (xcd >> 2) + (idx >> 3);
  unsigned long long* dbgp = (unsigned long long*)((bf16_t*)A2 + 9 * 1024 * 1024);
  STAMP(0);
  f4 acc[4][5];
#pragma unroll
  for (int r = 0; r < 4; ++r)
#pragma unroll
    for (int q = 0; q < 5; ++q) acc[r][q] = f4{0.f, 0.f, 0.f, 0.f};
  const char* ap[4];
#pragma unroll
  for (int r = 0; r < 4; ++r) ap[r] = (const char*)(A2 + ((size_t)((rb64 * 68 + w) * 4 + ((r + w) & 3)) * 64 + lane) * 8);
  const char* bp = (const char*)(W2f + ((size_t)(w * 40 + 5 * cb) * 64 + lane) * 8);
  u4 fa[PD][4], fb[PD][5];
  auto issue = [&](int i) {
    const char* b = bp + (size_t)i * 4 * 40 * 1024;
#pragma unroll
    for (int r = 0; r < 4; ++r)
      asm volatile("global_load_dwordx4 %0, %1, off" : "=v"(fa[i % PD][r]) : "v"(ap[r] + (size_t)i * 4 * 4096));
#pragma unroll
    for (int q = 0; q < 4; ++q)
      asm volatile("global_load_dwordx4 %0, %1, off offset:%2" : "=a"(fb[i % PD][q]) : "v"(b), "n"(q * 1024));
    asm volatile("global_load_dwordx4 %0, %1, off offset:%2" : "=a"(fb[i % PD][4]) : "v"(b + 4096), "n"(0));
  };
  const int grow = rb64 * 64 + 16 * w + (lane & 15), gcol = 80 * cb + 4 * (lane >> 4);
  u2 xv[5];
#pragma unroll
  for (int i = 0; i < PD - 1; ++i) issue(i);
#pragma unroll
  for (int i = 0; i < 17; ++i) {
    const int b = i % PD;
    if (i + PD - 1 < 17) issue(i + PD - 1);
    if (i + PD - 1 == 17) {
      const bf16_t* xp = x + grow * 640 + gcol;
#pragma unroll
      for (int ci = 0; ci < 5; ++ci)
        asm volatile("global_load_dwordx2 %0, %1, off offset:%2" : "=v"(xv[ci]) : "v"(xp), "n"(ci * 32));
    }
    const int rem = i + PD - 1 < 17 ? (PD - 1) * 9 : (16 - i) * 9 + 5;
    asm volatile("s_waitcnt vmcnt(%9)" : "+v"(fa[b][0]), "+v"(fa[b][1]), "+v"(fa[b][2]), "+v"(fa[b][3]),
                 "+a"(fb[b][0]), "+a"(fb[b][1]), "+a"(fb[b][2]), "+a"(fb[b][3]), "+a"(fb[b][4]) : "n"(rem) : "memory");
#pragma unroll
    for (int r = 3; r >= 0; --r)
#pragma unroll
      for (int ci = 0; ci < 5; ++ci) acc[r][ci] = MFMA(asbf(fb[b][ci]), asbf(fa[b][r]), acc[r][ci]);
  }
  STAMP(1);
#pragma unroll
  for (int r = 3; r >= 1; --r) {
    char* d = lds + ((((r + w) & 3) * 3 + r - 1) * 5) * 1024 + lane * 16;
#pragma unroll
    for (int q = 0; q < 5; ++q) *(f4*)(d + q * 1024) = acc[r][q];
  }
  LBAR();
  asm volatile("s_waitcnt vmcnt(0)" : "+v"(xv[0]), "+v"(xv[1]), "+v"(xv[2]), "+v"(xv[3]), "+v"(xv[4]) :: "memory");
  STAMP(2);
#pragma unroll
  for (int q = 0; q < 5; ++q) {
    f4 s = acc[0][q];
#pragma unroll
    for (int t = 0; t < 3; ++t) s += *(const f4*)(lds + ((w * 3 + t) * 5 + q) * 1024 + lane * 16);
    u2 o;
    o[0] = pk2(s[0] + lo16(xv[q][0]), s[1] + hi16(xv[q][0]));
    o[1] = pk2(s[2] + lo16(xv[q][1]), s[3] + hi16(xv[q][1]));
    *(u2*)(out + grow * 640 + gcol + 16 * q) = o;
  }
  STAMP(3);
}

extern "C" void kernel_launch(void* const* d_in, const int* in_sizes, int n_in, void* d_out, int out_size,
                              void* d_ws, size_t ws_size, hipStream_t stream) {
  const bf16_t* x = (const bf16_t*)d_in[0];
  const bf16_t* wqkv = (const bf16_t*)d_in[1];
  const bf16_t* wo = (const bf16_t*)d_in[2];
  const bf16_t* wg = (const bf16_t*)d_in[4];
  const bf16_t* wu = (const bf16_t*)d_in[5];
  const bf16_t* wd = (const bf16_t*)d_in[6];
  bf16_t* A2 = (bf16_t*)d_ws;
  bf16_t* W2f = A2 + 6 * 1024 * 1024;
  k1_kernel<<<256, 512, 0, stream>>>(x, wqkv, wg, wu, wo, wd, A2, W2f);
  k2_kernel<<<256, 256, 0, stream>>>(A2, W2f, x, (bf16_t*)d_out);
}
```

```cpp
#include <hip/hip_runtime.h>
#include <hip/hip_bf16.h>
#include <stdint.h>

typedef short v4s __attribute__((ext_vector_type(4)));
typedef __bf16 bf8 __attribute__((ext_vector_type(8)));
typedef __bf16 bf2 __attribute__((ext_vector_type(2)));
typedef float f4 __attribute__((ext_vector_type(4)));
typedef unsigned u4 __attribute__((ext_vector_type(4)));
typedef unsigned u2 __attribute__((ext_vector_type(2)));
typedef unsigned short bf16_t;

#define LAS __attribute__((address_space(3)))
#define GAS __attribute__((address_space(1)))
#define DEVI __device__ __forceinline__

__device__ const __attribute__((aligned(16))) unsigned g_ones[4] = {0x3F803F80u, 0x3F803F80u, 0x3F803F80u, 0x3F803F80u};

DEVI float bfr(float v) { return (float)(__bf16)v; }
DEVI unsigned pk2(float a, float b) { bf2 c; c[0] = (__bf16)a; c[1] = (__bf16)b; return __builtin_bit_cast(unsigned, c); }
DEVI float lo16(unsigned v) { return __builtin_bit_cast(float, v << 16); }
DEVI float hi16(unsigned v) { return __builtin_bit_cast(float, v & 0xffff0000u); }
DEVI float dot2(unsigned a, unsigned b, float c) {
  return __builtin_amdgcn_fdot2_f32_bf16(__builtin_bit_cast(bf2, a), __builtin_bit_cast(bf2, b), c, false);
}
DEVI void glds16(const void* g, unsigned ldsoff, char* lds) {
  __builtin_amdgcn_global_load_lds((const GAS void*)g, (LAS void*)(lds + ldsoff), 16, 0, 0);
}
#define RD128(d, addr, off) asm volatile("ds_read_b128 %0, %1 offset:%2" : "=v"(d) : "v"(addr), "n"(off))
#define RDTR(d, addr, off) asm volatile("ds_read_b64_tr_b16 %0, %1 offset:%2" : "=v"(d) : "v"(addr), "n"(off))
DEVI bf8 cat2(u2 a, u2 b) { u4 r; r[0] = a[0]; r[1] = a[1]; r[2] = b[0]; r[3] = b[1]; return __builtin_bit_cast(bf8, r); }
DEVI bf8 asbf(u4 a) { return __builtin_bit_cast(bf8, a); }
#define MFMA(a, b, c) __builtin_amdgcn_mfma_f32_16x16x32_bf16(a, b, c, 0, 0, 0)
#define WAITV(n) asm volatile("s_waitcnt vmcnt(" #n ")" ::: "memory")
#define WAITL0() asm volatile("s_waitcnt lgkmcnt(0)" ::: "memory")

#define ST_SC
#define ST8(p, v) asm volatile("global_store_dwordx2 %0, %1, off sc0 sc1" :: "v"(p), "v"(v) : "memory")
#define ST8O(p, v, o) asm volatile("global_store_dwordx2 %0, %1, off offset:%2 sc0 sc1" :: "v"(p), "v"(v), "n"(o) : "memory")
#define ST16(p, v) asm volatile("global_store_dwordx4 %0, %1, off sc0 sc1\n\ts_nop 1" :: "v"(p), "v"(v) : "memory")
#define GM 4
constexpr int ASLOT = 18432;
constexpr int BSLOT = 20480;
constexpr int BOFF = 3 * ASLOT;
constexpr int DUMMY_OFF = BOFF + 5 * BSLOT;
constexpr int IMG_STRIDE = 264;
constexpr int STAT_OFF = 144 * IMG_STRIDE;
#define STAMP(i)

DEVI unsigned a2_off(int grow, int kcol) {
  return (((((unsigned)grow >> 6) * 68 + ((unsigned)kcol >> 5)) * 4 + (((unsigned)grow >> 4) & 3)) * 64 +
          (((((unsigned)kcol >> 3) & 3) << 4) | ((unsigned)grow & 15))) * 8 + ((unsigned)kcol & 7);
}

struct K1Ctx {
  unsigned lo[5], hi[5];
  unsigned str[5];
  unsigned pdst[5];
  unsigned kind[5];
  const char* pf;
  unsigned pfs;
};
DEVI void k1_pf(K1Ctx& c, char* lds) {
  glds16(c.pf, DUMMY_OFF, lds);
  c.pf += c.pfs;
}

DEVI void k1_piece(K1Ctx& c, char* lds, int i, unsigned sb, unsigned sa, bool live, bool oddg) {
  const bool lv = (i == 2 && oddg) ? (live && c.kind[2] == 0) : live;
  const char* p = lv ? (const char*)(((uint64_t)c.hi[i] << 32) | c.lo[i]) : (const char*)g_ones;
  unsigned d;
  if (i < 2) d = sb + c.pdst[i];
  else if (i > 2) d = sa + c.pdst[i];
  else if (oddg) d = c.kind[2] == 0 ? sb + c.pdst[2] : (unsigned)DUMMY_OFF;
  else d = (c.kind[2] == 0 ? sb : c.kind[2] == 1 ? sa : 0u) + c.pdst[2];
  glds16(p, d, lds);
  asm("v_add_co_u32 %0, vcc, %0, %2\n\tv_addc_co_u32 %1, vcc, 0, %1, vcc" : "+v"(c.lo[i]), "+v"(c.hi[i]) : "v"(c.str[i]) : "vcc");
}
#define LBAR() do { asm volatile("s_waitcnt lgkmcnt(0)" ::: "memory"); __builtin_amdgcn_s_barrier(); asm volatile("" ::: "memory"); } while (0)

template <int MH, int NP>
DEVI void k1_main(K1Ctx& c, char* lds, int lane, int lst, int r0, int cg, bf16_t* __restrict__ A2,
                  unsigned long long* dbgp) {
  constexpr int NRT = MH == 0 ? 5 : 4;
  constexpr int RT0 = MH == 0 ? 0 : 5;
  constexpr int NC = 2 + 2 * NP;
  constexpr int NG = NP == 2 ? 0 : (MH == 0 ? 3 : 2);
  constexpr int NA = NRT + NG;
  f4 acc[NRT][NC];
  f4 gacc[NG + 1];
#pragma unroll
  for (int r = 0; r < NRT; ++r)
#pragma unroll
    for (int q = 0; q < NC; ++q) acc[r][q] = f4{0.f, 0.f, 0.f, 0.f};
#pragma unroll
  for (int r = 0; r < NG + 1; ++r) gacc[r] = f4{0.f, 0.f, 0.f, 0.f};
  const unsigned lbase = (unsigned)(uintptr_t)(LAS char*)lds;
  const int ar = lane & 15, ac = lane >> 4;
  const unsigned abase0 = lbase + ar * 128 + ((ac ^ (ar & 7)) * 16);
  const unsigned abase1 = lbase + ar * 128 + (((4 + ac) ^ (ar & 7)) * 16);
  const int gt0 = MH == 0 ? 3 * (lst - 2) : 6 + (lst - 2);
  const int g = lane >> 4, q4 = (lane & 15) >> 2, p4 = lane & 3;
  const int p0 = NP == 2 ? 2 * lst : lst + 2;
  const unsigned hx = (((q4 >> 1) & 1) | ((g & 1) << 1)) << 5;
  auto baddr = [&](int ct) { return lbase + BOFF + ((((8 * g + q4) * 640 + 32 * ct) ^ hx) + 8 * p4); };
  unsigned bb[NC];
  bb[0] = baddr(2 * lst); bb[1] = baddr(2 * lst + 1);
#pragma unroll
  for (int p = 0; p < NP; ++p) { bb[2 + p] = baddr(8 + p0 + p); bb[2 + NP + p] = baddr(14 + p0 + p); }
  u4 fa[2][NA];
  u2 fb[2][NC][2];
  unsigned a0, g0, bc[NC];
  auto bases = [&](int odd, unsigned sb, unsigned sa) {
    a0 = (odd ? abase1 : abase0) + sa; g0 = a0 + gt0 * 2048;
#pragma unroll
    for (int ci = 0; ci < NC; ++ci) bc[ci] = bb[ci] + sb;
  };
  auto memop = [&](int nb, int k, int npc, unsigned sb, unsigned sa, bool live, bool liveA) {
    if (k < 2 * NC) {
      RDTR(fb[nb][k >> 1][k & 1], bc[k >> 1], (k & 1) * 2560);
    } else if (k < 2 * NC + NA) {
      const int r = k - 2 * NC;
      if (r < NRT) RD128(fa[nb][r], a0, (RT0 + r) * 2048);
      else RD128(fa[nb][r], g0, (r - NRT) * 2048);
    } else if (k < 2 * NC + NA + npc) {
      const int i = k - 2 * NC - NA;
      k1_piece(c, lds, i, sb, sa, live, npc == 3);
    }
  };
  auto wait_frags = [&](int buf) {
    asm volatile("s_waitcnt lgkmcnt(0)"
                 : "+v"(fa[buf][0]), "+v"(fa[buf][1]), "+v"(fa[buf][2]), "+v"(fa[buf][3]),
                   "+v"(fb[buf][0][0]), "+v"(fb[buf][0][1]), "+v"(fb[buf][1][0]), "+v"(fb[buf][1][1]), "+v"(fb[buf][2][0]),
                   "+v"(fb[buf][2][1]), "+v"(fb[buf][3][0]), "+v"(fb[buf][3][1])
                 :: "memory");
#pragma unroll
    for (int r = 4; r < NA; ++r) asm volatile("" : "+v"(fa[buf][r]) :: "memory");
#pragma unroll
    for (int q = 4; q < NC; ++q) asm volatile("" : "+v"(fb[buf][q][0]), "+v"(fb[buf][q][1]) :: "memory");
  };
  WAITV(10);
  __builtin_amdgcn_s_barrier();
  STAMP(1);
  bases(0, 0, 0);
#pragma unroll
  for (int k = 0; k < 2 * NC + NA; ++k) memop(0, k, 0, 0, 0, true, true);
  unsigned sw = 0;
  unsigned sa = 0;
  auto step = [&](int b, bool first, bool live, unsigned san, bool second = false) {
    const unsigned sn = (sw == 4 * BSLOT) ? 0u : sw + BSLOT;
    const unsigned sap = (sa == 0) ? 2 * ASLOT : sa - ASLOT;
    const unsigned sp = sw >= 2 * BSLOT ? sw - 2 * BSLOT : sw + 3 * BSLOT;
    bases(b ^ 1, sn, b ? san : sa);
    if (first) WAITV(6); else if (second) WAITV(6); else if (b == 0) WAITV(5); else WAITV(3);
    wait_frags(b);
    __builtin_amdgcn_s_barrier();
    const int npc = b ? 5 : 3;
    auto mo = [&](int k) {
      if (first && k >= 2 * NC + NA) {
        const int i = k - 2 * NC - NA;
        if (i < 3) k1_piece(c, lds, i, 3 * BSLOT, 0, true, true);
        else if (i < 6) k1_pf(c, lds);
      } else {
        if (first) memop(b ^ 1, k, npc, sp, sap, live, live);
        else if ((k % GM) == GM - 1 && (k / GM) < npc) memop(b ^ 1, 2 * NC + NA + (k / GM), npc, sp, sap, live, live);
        else { const int kr = k - ((k / GM) < npc ? (k / GM) : npc); if (kr < 2 * NC + NA) memop(b ^ 1, kr, npc, sp, sap, live, live); }
      }
    };
    int k = 0;
#pragma unroll
    for (int ci = 0; ci < NC; ++ci)
#pragma unroll
      for (int r = 0; r < NRT; ++r) {
        acc[r][ci] = MFMA(cat2(fb[b][ci][0], fb[b][ci][1]), asbf(fa[b][r]), acc[r][ci]);
        mo(k); ++k;
        __builtin_amdgcn_sched_barrier(0);
      }
#pragma unroll
    for (int r = 0; r < NG; ++r) {
      gacc[r] = MFMA(asbf(fa[b][NRT + r]), asbf(fa[b][NRT + r]), gacc[r]);
      mo(k); ++k;
      __builtin_amdgcn_sched_barrier(0);
    }
#pragma unroll
    for (int kk = NC * NRT + NG; kk < 2 * NC + NA + 14; ++kk) mo(kk);
    sw = sn;
  };
  step(0, true, true, ASLOT);
  step(1, false, true, ASLOT, true);
  sa = ASLOT;
#pragma unroll 1
  for (int s = 2; s < 20; s += 2) {
    const unsigned san = (sa == 2 * ASLOT) ? 0u : sa + ASLOT;
    step(0, false, s < 17, san);
    step(1, false, s + 1 < 17, san);
    sa = san;
  }
  asm volatile("s_waitcnt vmcnt(0) lgkmcnt(0)" ::: "memory");
  __builtin_amdgcn_s_barrier();
  STAMP(2);
  float* st_rs = (float*)(lds + STAT_OFF);
  float* st_sq = st_rs + 144;
  float* st_cs = st_sq + 144;
  if constexpr (NG > 0) {
    const int rr = lane & 3;
    if (((lane & 15) >> 2) == (lane >> 4)) {
#pragma unroll
      for (int r = 0; r < NG; ++r)
        st_sq[16 * (gt0 + r) + (lane & 15)] = rr == 0 ? gacc[r][0] : rr == 1 ? gacc[r][1] : rr == 2 ? gacc[r][2] : gacc[r][3];
    }
    if (lst == 3 && (lane >> 4) == 2) {
#pragma unroll
      for (int r = 0; r < NRT; ++r) st_rs[16 * (RT0 + r) + (lane & 15)] = acc[r][1][0];
    }
  }
  int ctile[NC];
  ctile[0] = 2 * lst; ctile[1] = 2 * lst + 1;
#pragma unroll
  for (int p = 0; p < NP; ++p) { ctile[2 + p] = 8 + p0 + p; ctile[2 + NP + p] = 14 + p0 + p; }
  if constexpr (MH == 0) {
    if ((lane & 15) == 0) {
#pragma unroll
      for (int ci = 0; ci < NC; ++ci) *(f4*)(st_cs + 16 * ctile[ci] + 4 * (lane >> 4)) = acc[0][ci];
    }
  }
  LBAR();
  f4 cs[NC];
  bf16_t* hp[NP];
#pragma unroll
  for (int p = 0; p < NP; ++p) hp[p] = A2 + a2_off(r0 + (lane & 15), 640 + 96 * cg + 16 * (p0 + p) + 4 * (lane >> 4));
#pragma unroll
  for (int ci = 0; ci < NC; ++ci) cs[ci] = *(const f4*)(st_cs + 16 * ctile[ci] + 4 * (lane >> 4));
  float nm[NRT], sd[NRT], rinv[NRT];
#pragma unroll
  for (int r = 0; r < NRT; ++r) {
    const int row = 16 * (RT0 + r) + (lane & 15);
    const float mean = st_rs[row] * (1.0f / 640.0f);
    const float var = fmaxf(st_sq[row] * (1.0f / 640.0f) - mean * mean, 0.f);
    nm[r] = -bfr(mean);
    sd[r] = bfr(__builtin_amdgcn_sqrtf(bfr(bfr(var) + 1e-5f)));
    rinv[r] = __builtin_amdgcn_rcpf(sd[r]);
  }
#pragma unroll
  for (int r = 0; r < NRT; ++r) {
    const int row = 16 * (RT0 + r) + (lane & 15);
    const f4 nm4 = {nm[r], nm[r], nm[r], nm[r]};
#pragma unroll
    for (int ci = 0; ci < 2; ++ci) {
      f4 y = __builtin_elementwise_fma(cs[ci], nm4, acc[r][ci]) * rinv[r];
      u2 o; o[0] = pk2(y[0], y[1]); o[1] = pk2(y[2], y[3]);
      *(u2*)(lds + row * IMG_STRIDE + (16 * ctile[ci] + 4 * (lane >> 4)) * 2) = o;
    }
  }
  constexpr int R1 = MH == 0 ? 1 : 0;
#pragma unroll
  for (int r = R1; r < NRT; ++r) {
    const f4 nm4 = {nm[r], nm[r], nm[r], nm[r]};
    const float ga = -1.4426950408889634f * rinv[r];
#pragma unroll
    for (int p = 0; p < NP; ++p) {
      acc[r][2 + p] = __builtin_elementwise_fma(cs[2 + p], nm4, acc[r][2 + p]) * ga;
      acc[r][2 + NP + p] = __builtin_elementwise_fma(cs[2 + NP + p], nm4, acc[r][2 + NP + p]);
    }
  }
#pragma unroll
  for (int r = R1; r < NRT; ++r)
#pragma unroll
    for (int p = 0; p < NP; ++p)
#pragma unroll
      for (int e = 0; e < 4; ++e) acc[r][2 + p][e] = __builtin_amdgcn_exp2f(acc[r][2 + p][e]);
#pragma unroll
  for (int r = R1; r < NRT; ++r) {
    const f4 sd4 = {sd[r], sd[r], sd[r], sd[r]};
#pragma unroll
    for (int p = 0; p < NP; ++p) acc[r][2 + p] = __builtin_elementwise_fma(acc[r][2 + p], sd4, sd4);
  }
#pragma unroll
  for (int r = R1; r < NRT; ++r)
#pragma unroll
    for (int p = 0; p < NP; ++p)
#pragma unroll
      for (int e = 0; e < 4; ++e) acc[r][2 + p][e] = __builtin_amdgcn_rcpf(acc[r][2 + p][e]);
#pragma unroll
  for (int r = R1; r < NRT; ++r) {
    const int rt = RT0 + r - 1;
#pragma unroll
    for (int p = 0; p < NP; ++p) {
      f4 d = acc[r][2 + p] * acc[r][2 + NP + p];
      u2 o; o[0] = pk2(d[0], d[1]); o[1] = pk2(d[2], d[3]);
      ST8O(hp[p] + (rt >> 2) * (68 * 4 * 512), o, (rt & 3) * 1024);
    }
  }
}

DEVI float xor1(float v) { return __builtin_bit_cast(float, __builtin_amdgcn_mov_dpp(__builtin_bit_cast(int, v), 0xB1, 0xf, 0xf, true)); }

__global__ __launch_bounds__(512, 2) void k1_kernel(const bf16_t* __restrict__ x, const bf16_t* __restrict__ wqkv,
                                                    const bf16_t* __restrict__ wg, const bf16_t* __restrict__ wu,
                                                    const bf16_t* __restrict__ wo, const bf16_t* __restrict__ wd,
                                                    bf16_t* __restrict__ A2, bf16_t* __restrict__ W2f) {
  __shared__ __attribute__((aligned(16))) char lds[DUMMY_OFF + 1024];
  const int tid = threadIdx.x, lane = tid & 63;
  const int w = __builtin_amdgcn_readfirstlane(tid >> 6);
  const int bid = blockIdx.x, xcd = bid & 7, idx = bid >> 3;
  const int rb = 8 * (xcd & 1) + (idx & 7), cg = 4 * (xcd >> 1) + (idx >> 3);
  const int r0 = rb * 128;
  unsigned long long* dbgp = (unsigned long long*)(A2 + 8 * 1024 * 1024);
  STAMP(0);
  K1Ctx c;
  {
#pragma unroll
    for (int i = 0; i < 5; ++i) {
      const char* src = (const char*)g_ones; unsigned str = 0, kind = 2, dst = DUMMY_OFF;
      const int pb = w + 8 * i;
      const int pa = i == 2 ? 12 + w : w + 8 * (i - 3);
      if (i < 3 && pb < 20) {
        const unsigned ap = pb * 1024 + lane * 16;
        const unsigned k = ap / 640;
        const unsigned al = ap ^ ((((k >> 1) & 1) | (((k >> 3) & 1) << 1)) << 5);
        const unsigned ch = (al - k * 640) >> 4;
        kind = 0; dst = BOFF + pb * 1024;
        if (ch < 15) {
          src = (const char*)(wqkv + k * 1920 + ((ch * 13) >> 6) * 600 + 40 * cg + 8 * ch); str = 32u * 1920 * 2;
        } else if (ch > 15) {
          src = (const char*)((ch < 28 ? wg + 8 * (ch - 16) : wu + 8 * (ch - 28)) + k * 1536 + 96 * cg); str = 32u * 1536 * 2;
        }
      } else if (i >= 3 || (i == 2 && w < 6)) {
        const int lr = 8 * pa + (lane >> 3);
        int grow = r0 - 16 + lr;
        grow = grow < 0 ? 0 : grow;
        kind = 1; dst = pa * 1024;
        if (lr != 0) { src = (const char*)(x + grow * 640 + (((lane & 7) ^ (lr & 7)) * 8)); str = i == 2 ? 64u : 128u; }
      }
      c.lo[i] = (unsigned)(uint64_t)src; c.hi[i] = (unsigned)((uint64_t)src >> 32); c.str[i] = str;
      c.pdst[i] = dst; c.kind[i] = kind;
    }
  }
  {
    const int gw = bid * 8 + w;
    const char* pb = (const char*)g_ones; unsigned per = 0; int id = 0;
    if (gw < 400) { pb = (const char*)wqkv; per = 400; id = gw; }
    else if (gw < 720) { pb = (const char*)wg; per = 320; id = gw - 400; }
    else if (gw < 1040) { pb = (const char*)wu; per = 320; id = gw - 720; }
    else if (gw < 1466) { pb = (const char*)x; per = 426; id = gw - 1040; }
    else if (gw < 1599) { pb = (const char*)wo; per = 133; id = gw - 1466; }
    else if (gw < 1919) { pb = (const char*)wd; per = 320; id = gw - 1599; }
    c.pf = per ? pb + id * 1024 + lane * 16 : pb;
    c.pfs = per * 1024;
  }
  STAMP(5);
#pragma unroll
  for (int s = 0; s < 3; ++s) {
#pragma unroll
    for (int i = 0; i < ((s & 1) ? 3 : 5); ++i) k1_piece(c, lds, i, s * BSLOT, (s >> 1) * ASLOT, true, (s & 1) != 0);
    k1_pf(c, lds);
    if (s < 2) __builtin_amdgcn_s_barrier();
  }
  STAMP(6);
  {
    const int lst = w < 4 ? w : ((w + 2) & 3);
    if (w < 2) k1_main<0, 2>(c, lds, lane, lst, r0, cg, A2, dbgp);
    else if (w < 4) k1_main<0, 1>(c, lds, lane, lst, r0, cg, A2, dbgp);
    else if (w < 6) k1_main<1, 1>(c, lds, lane, lst, r0, cg, A2, dbgp);
    else k1_main<1, 2>(c, lds, lane, lst, r0, cg, A2, dbgp);
  }
  const int gw = bid * 8 + w;
  unsigned short wv[2][8];
#pragma unroll
  for (int t = 0; t < 2; ++t) {
    const int fi = gw + 2048 * t;
    if (t == 0 || fi < 2720) {
      const int ks = fi / 40, ct = fi - ks * 40;
      const bf16_t* p = (ks < 20 ? wo + ks * 32 * 640 : wd + (ks - 20) * 32 * 640) + (8 * (lane >> 4)) * 640 + 16 * ct + (lane & 15);
#pragma unroll
      for (int e = 0; e < 8; ++e) wv[t][e] = p[e * 640];
    }
  }
  LBAR();
  STAMP(3);
  {
    const int half = tid & 1, pi = tid >> 1, hh = pi >> 7, row = pi & 127, pos = r0 + row;
    const char* pr = lds + (16 + row) * IMG_STRIDE + 40 * hh;
    const char* pw = pr - 8 * half * IMG_STRIDE;
    unsigned qv[10];
#pragma unroll
    for (int i = 0; i < 5; ++i) { u2 t = *(const u2*)(pr + 8 * i); qv[2 * i] = t[0]; qv[2 * i + 1] = t[1]; }
    float lg[4];
#pragma unroll
    for (int ww = 0; ww < 4; ++ww) {
      const char* pk = pw - 2 * ww * IMG_STRIDE + 80;
      float a = 0.f;
#pragma unroll
      for (int i = 0; i < 5; ++i) {
        u2 t = *(const u2*)(pk + 8 * i);
        a = dot2(qv[2 * i], t[0], a); a = dot2(qv[2 * i + 1], t[1], a);
      }
      a = bfr(bfr(a) * 0.22360679774997896f);
      lg[ww] = (pos - 2 * (4 * half + ww) >= 0) ? a : -1e9f;
    }
    float mx = fmaxf(fmaxf(lg[0], lg[1]), fmaxf(lg[2], lg[3]));
    mx = fmaxf(mx, xor1(mx));
    float sum = 0.f;
#pragma unroll
    for (int ww = 0; ww < 4; ++ww) { lg[ww] = __expf(lg[ww] - mx); sum += lg[ww]; }
    sum += xor1(sum);
    const float inv = __builtin_amdgcn_rcpf(sum);
    float o[20];
#pragma unroll
    for (int i = 0; i < 20; ++i) o[i] = 0.f;
#pragma unroll
    for (int wp = 0; wp < 2; ++wp) {
      const unsigned P = pk2(lg[2 * wp] * inv, lg[2 * wp + 1] * inv);
      const char* pv0 = pw - 4 * wp * IMG_STRIDE + 160;
      const char* pv1 = pv0 - 2 * IMG_STRIDE;
#pragma unroll
      for (int i = 0; i < 5; ++i) {
        u2 a = *(const u2*)(pv0 + 8 * i), b = *(const u2*)(pv1 + 8 * i);
#pragma unroll
        for (int e = 0; e < 2; ++e) {
          unsigned lo = __builtin_amdgcn_perm(b[e], a[e], 0x05040100u);
          unsigned hi = __builtin_amdgcn_perm(b[e], a[e], 0x07060302u);
          o[4 * i + 2 * e] = dot2(lo, P, o[4 * i + 2 * e]);
          o[4 * i + 2 * e + 1] = dot2(hi, P, o[4 * i + 2 * e + 1]);
        }
      }
    }
#pragma unroll
    for (int i = 0; i < 20; ++i) o[i] += xor1(o[i]);
    const int kc0 = 40 * cg + 20 * hh;
#pragma unroll
    for (int i = 0; i < 3; ++i) {
      const int pc = half ? 2 + i : i;
      u2 t;
      t[0] = half ? pk2(o[4 * (2 + i)], o[4 * (2 + i) + 1]) : pk2(o[4 * i], o[4 * i + 1]);
      t[1] = half ? pk2(o[4 * (2 + i) + 2], o[4 * (2 + i) + 3]) : pk2(o[4 * i + 2], o[4 * i + 3]);
      ST8(A2 + a2_off(pos, kc0 + 4 * pc), t);
    }
  }
#pragma unroll
  for (int t = 0; t < 2; ++t) {
    const int fi = gw + 2048 * t;
    if (t == 0 || fi < 2720) {
      u4 o;
#pragma unroll
      for (int e = 0; e < 4; ++e) o[e] = (unsigned)wv[t][2 * e] | ((unsigned)wv[t][2 * e + 1] << 16);
      ST16(W2f + ((size_t)fi * 64 + lane) * 8, o);
    }
  }
  STAMP(4);
}

#define PDV 2
constexpr int PD = PDV;

__global__ __launch_bounds__(256, 1) void k2_kernel(const bf16_t* __restrict__ A2, const bf16_t* __restrict__ W2f,
                                                    const bf16_t* __restrict__ x, bf16_t* __restrict__ out) {
  __shared__ __attribute__((aligned(16))) char lds[4 * 15 * 1024];
  const int tid = threadIdx.x, lane = tid & 63;
  const int w = __builtin_amdgcn_readfirstlane(tid >> 6);
  const int bid = blockIdx.x, xcd = bid & 7, idx = bid >> 3;
  const int rb64 = 8 * (xcd & 3) + (idx & 7), cb = 4 * (xcd >> 2) + (idx >> 3);
  unsigned long long* dbgp = (unsigned long long*)((bf16_t*)A2 + 9 * 1024 * 1024);
  STAMP(0);
  f4 acc[4][5];
#pragma unroll
  for (int r = 0; r < 4; ++r)
#pragma unroll
    for (int q = 0; q < 5; ++q) acc[r][q] = f4{0.f, 0.f, 0.f, 0.f};
  const char* ap[4];
#pragma unroll
  for (int r = 0; r < 4; ++r) ap[r] = (const char*)(A2 + ((size_t)((rb64 * 68 + w) * 4 + ((r + w) & 3)) * 64 + lane) * 8);
  const char* bp = (const char*)(W2f + ((size_t)(w * 40 + 5 * cb) * 64 + lane) * 8);
  u4 fa[PD][4], fb[PD][5];
  auto issue = [&](int i) {
    const char* b = bp + (size_t)i * 4 * 40 * 1024;
#pragma unroll
    for (int r = 0; r < 4; ++r)
      asm volatile("global_load_dwordx4 %0, %1, off" : "=v"(fa[i % PD][r]) : "v"(ap[r] + (size_t)i * 4 * 4096));
#pragma unroll
    for (int q = 0; q < 4; ++q)
      asm volatile("global_load_dwordx4 %0, %1, off offset:%2" : "=a"(fb[i % PD][q]) : "v"(b), "n"(q * 1024));
    asm volatile("global_load_dwordx4 %0, %1, off offset:%2" : "=a"(fb[i % PD][4]) : "v"(b + 4096), "n"(0));
  };
  const int grow = rb64 * 64 + 16 * w + (lane & 15), gcol = 80 * cb + 4 * (lane >> 4);
  u2 xv[5];
#pragma unroll
  for (int i = 0; i < PD - 1; ++i) issue(i);
#pragma unroll
  for (int i = 0; i < 17; ++i) {
    const int b = i % PD;
    if (i + PD - 1 < 17) issue(i + PD - 1);
    if (i + PD - 1 == 17) {
      const bf16_t* xp = x + grow * 640 + gcol;
#pragma unroll
      for (int ci = 0; ci < 5; ++ci)
        asm volatile("global_load_dwordx2 %0, %1, off offset:%2" : "=v"(xv[ci]) : "v"(xp), "n"(ci * 32));
    }
    const int rem = i + PD - 1 < 17 ? (PD - 1) * 9 : (16 - i) * 9 + 5;
    asm volatile("s_waitcnt vmcnt(%9)" : "+v"(fa[b][0]), "+v"(fa[b][1]), "+v"(fa[b][2]), "+v"(fa[b][3]),
                 "+a"(fb[b][0]), "+a"(fb[b][1]), "+a"(fb[b][2]), "+a"(fb[b][3]), "+a"(fb[b][4]) : "n"(rem) : "memory");
#pragma unroll
    for (int r = 3; r >= 0; --r)
#pragma unroll
      for (int ci = 0; ci < 5; ++ci) acc[r][ci] = MFMA(asbf(fb[b][ci]), asbf(fa[b][r]), acc[r][ci]);
  }
  STAMP(1);
#pragma unroll
  for (int r = 3; r >= 1; --r) {
    char* d = lds + ((((r + w) & 3) * 3 + r - 1) * 5) * 1024 + lane * 16;
#pragma unroll
    for (int q = 0; q < 5; ++q) *(f4*)(d + q * 1024) = acc[r][q];
  }
  LBAR();
  asm volatile("s_waitcnt vmcnt(0)" : "+v"(xv[0]), "+v"(xv[1]), "+v"(xv[2]), "+v"(xv[3]), "+v"(xv[4]) :: "memory");
  STAMP(2);
#pragma unroll
  for (int q = 0; q < 5; ++q) {
    f4 s = acc[0][q];
#pragma unroll
    for (int t = 0; t < 3; ++t) s += *(const f4*)(lds + ((w * 3 + t) * 5 + q) * 1024 + lane * 16);
    u2 o;
    o[0] = pk2(s[0] + lo16(xv[q][0]), s[1] + hi16(xv[q][0]));
    o[1] = pk2(s[2] + lo16(xv[q][1]), s[3] + hi16(xv[q][1]));
    *(u2*)(out + grow * 640 + gcol + 16 * q) = o;
  }
  STAMP(3);
}

extern "C" void kernel_launch(void* const* d_in, const int* in_sizes, int n_in, void* d_out, int out_size,
                              void* d_ws, size_t ws_size, hipStream_t stream) {
  const bf16_t* x = (const bf16_t*)d_in[0];
  const bf16_t* wqkv = (const bf16_t*)d_in[1];
  const bf16_t* wo = (const bf16_t*)d_in[2];
  const bf16_t* wg = (const bf16_t*)d_in[4];
  const bf16_t* wu = (const bf16_t*)d_in[5];
  const bf16_t* wd = (const bf16_t*)d_in[6];
  bf16_t* A2 = (bf16_t*)d_ws;
  bf16_t* W2f = A2 + 6 * 1024 * 1024;
  k1_kernel<<<256, 512, 0, stream>>>(x, wqkv, wg, wu, wo, wd, A2, W2f);
  k2_kernel<<<256, 256, 0, stream>>>(A2, W2f, x, (bf16_t*)d_out);
}
```

```cpp
#define NPF0 3
#include <hip/hip_runtime.h>
#include <hip/hip_bf16.h>
#include <stdint.h>

typedef short v4s __attribute__((ext_vector_type(4)));
typedef __bf16 bf8 __attribute__((ext_vector_type(8)));
typedef __bf16 bf2 __attribute__((ext_vector_type(2)));
typedef float f4 __attribute__((ext_vector_type(4)));
typedef unsigned u4 __attribute__((ext_vector_type(4)));
typedef unsigned u2 __attribute__((ext_vector_type(2)));
typedef unsigned short bf16_t;

#define LAS __attribute__((address_space(3)))
#define GAS __attribute__((address_space(1)))
#define DEVI __device__ __forceinline__

__device__ const __attribute__((aligned(16))) unsigned g_ones[4] = {0x3F803F80u, 0x3F803F80u, 0x3F803F80u, 0x3F803F80u};

DEVI float bfr(float v) { return (float)(__bf16)v; }
DEVI unsigned pk2(float a, float b) { bf2 c; c[0] = (__bf16)a; c[1] = (__bf16)b; return __builtin_bit_cast(unsigned, c); }
DEVI float lo16(unsigned v) { return __builtin_bit_cast(float, v << 16); }
DEVI float hi16(unsigned v) { return __builtin_bit_cast(float, v & 0xffff0000u); }
DEVI float dot2(unsigned a, unsigned b, float c) {
  return __builtin_amdgcn_fdot2_f32_bf16(__builtin_bit_cast(bf2, a), __builtin_bit_cast(bf2, b), c, false);
}
DEVI void glds16(const void* g, unsigned ldsoff, char* lds) {
  __builtin_amdgcn_global_load_lds((const GAS void*)g, (LAS void*)(lds + ldsoff), 16, 0, 0);
}
#define RD128(d, addr, off) asm volatile("ds_read_b128 %0, %1 offset:%2" : "=v"(d) : "v"(addr), "n"(off))
#define RDTR(d, addr, off) asm volatile("ds_read_b64_tr_b16 %0, %1 offset:%2" : "=v"(d) : "v"(addr), "n"(off))
DEVI bf8 cat2(u2 a, u2 b) { u4 r; r[0] = a[0]; r[1] = a[1]; r[2] = b[0]; r[3] = b[1]; return __builtin_bit_cast(bf8, r); }
DEVI bf8 asbf(u4 a) { return __builtin_bit_cast(bf8, a); }
#define MFMA(a, b, c) __builtin_amdgcn_mfma_f32_16x16x32_bf16(a, b, c, 0, 0, 0)
#define WAITV(n) asm volatile("s_waitcnt vmcnt(" #n ")" ::: "memory")
#define WAITL0() asm volatile("s_waitcnt lgkmcnt(0)" ::: "memory")

#define ST_SC
#define ST8(p, v) asm volatile("global_store_dwordx2 %0, %1, off sc0 sc1" :: "v"(p), "v"(v) : "memory")
#define ST8O(p, v, o) asm volatile("global_store_dwordx2 %0, %1, off offset:%2 sc0 sc1" :: "v"(p), "v"(v), "n"(o) : "memory")
#define ST16(p, v) asm volatile("global_store_dwordx4 %0, %1, off sc0 sc1\n\ts_nop 1" :: "v"(p), "v"(v) : "memory")
#define GM 4
constexpr int ASLOT = 18432;
constexpr int BSLOT = 20480;
constexpr int BOFF = 3 * ASLOT;
constexpr int DUMMY_OFF = BOFF + 5 * BSLOT;
constexpr int IMG_STRIDE = 264;
constexpr int STAT_OFF = 144 * IMG_STRIDE;
#define STAMP(i)

DEVI unsigned a2_off(int grow, int kcol) {
  return (((((unsigned)grow >> 6) * 68 + ((unsigned)kcol >> 5)) * 4 + (((unsigned)grow >> 4) & 3)) * 64 +
          (((((unsigned)kcol >> 3) & 3) << 4) | ((unsigned)grow & 15))) * 8 + ((unsigned)kcol & 7);
}

struct K1Ctx {
  unsigned lo[5], hi[5];
  unsigned str[5];
  unsigned pdst[5];
  unsigned kind[5];
  const char* pf;
  unsigned pfs;
};
DEVI void k1_pf(K1Ctx& c, char* lds) {
  glds16(c.pf, DUMMY_OFF, lds);
  c.pf += c.pfs;
}

DEVI void k1_piece(K1Ctx& c, char* lds, int i, unsigned sb, unsigned sa, bool live, bool oddg) {
  const bool lv = (i == 2 && oddg) ? (live && c.kind[2] == 0) : live;
  const char* p = lv ? (const char*)(((uint64_t)c.hi[i] << 32) | c.lo[i]) : (const char*)g_ones;
  unsigned d;
  if (i < 2) d = sb + c.pdst[i];
  else if (i > 2) d = sa + c.pdst[i];
  else if (oddg) d = c.kind[2] == 0 ? sb + c.pdst[2] : (unsigned)DUMMY_OFF;
  else d = (c.kind[2] == 0 ? sb : c.kind[2] == 1 ? sa : 0u) + c.pdst[2];
  glds16(p, d, lds);
  asm("v_add_co_u32 %0, vcc, %0, %2\n\tv_addc_co_u32 %1, vcc, 0, %1, vcc" : "+v"(c.lo[i]), "+v"(c.hi[i]) : "v"(c.str[i]) : "vcc");
}
#define LBAR() do { asm volatile("s_waitcnt lgkmcnt(0)" ::: "memory"); __builtin_amdgcn_s_barrier(); asm volatile("" ::: "memory"); } while (0)

template <int MH, int NP>
DEVI void k1_main(K1Ctx& c, char* lds, int lane, int lst, int r0, int cg, bf16_t* __restrict__ A2,
                  unsigned long long* dbgp) {
  constexpr int NRT = MH == 0 ? 5 : 4;
  constexpr int RT0 = MH == 0 ? 0 : 5;
  constexpr int NC = 2 + 2 * NP;
  constexpr int NG = NP == 2 ? 0 : (MH == 0 ? 3 : 2);
  constexpr int NA = NRT + NG;
  f4 acc[NRT][NC];
  f4 gacc[NG + 1];
#pragma unroll
  for (int r = 0; r < NRT; ++r)
#pragma unroll
    for (int q = 0; q < NC; ++q) acc[r][q] = f4{0.f, 0.f, 0.f, 0.f};
#pragma unroll
  for (int r = 0; r < NG + 1; ++r) gacc[r] = f4{0.f, 0.f, 0.f, 0.f};
  const unsigned lbase = (unsigned)(uintptr_t)(LAS char*)lds;
  const int ar = lane & 15, ac = lane >> 4;
  const unsigned abase0 = lbase + ar * 128 + ((ac ^ (ar & 7)) * 16);
  const unsigned abase1 = lbase + ar * 128 + (((4 + ac) ^ (ar & 7)) * 16);
  const int gt0 = MH == 0 ? 3 * (lst - 2) : 6 + (lst - 2);
  const int g = lane >> 4, q4 = (lane & 15) >> 2, p4 = lane & 3;
  const int p0 = NP == 2 ? 2 * lst : lst + 2;
  const unsigned hx = (((q4 >> 1) & 1) | ((g & 1) << 1)) << 5;
  auto baddr = [&](int ct) { return lbase + BOFF + ((((8 * g + q4) * 640 + 32 * ct) ^ hx) + 8 * p4); };
  unsigned bb[NC];
  bb[0] = baddr(2 * lst); bb[1] = baddr(2 * lst + 1);
#pragma unroll
  for (int p = 0; p < NP; ++p) { bb[2 + p] = baddr(8 + p0 + p); bb[2 + NP + p] = baddr(14 + p0 + p); }
  u4 fa[2][NA];
  u2 fb[2][NC][2];
  unsigned a0, g0, bc[NC];
  auto bases = [&](int odd, unsigned sb, unsigned sa) {
    a0 = (odd ? abase1 : abase0) + sa; g0 = a0 + gt0 * 2048;
#pragma unroll
    for (int ci = 0; ci < NC; ++ci) bc[ci] = bb[ci] + sb;
  };
  auto memop = [&](int nb, int k, int npc, unsigned sb, unsigned sa, bool live, bool liveA) {
    if (k < 2 * NC) {
      RDTR(fb[nb][k >> 1][k & 1], bc[k >> 1], (k & 1) * 2560);
    } else if (k < 2 * NC + NA) {
      const int r = k - 2 * NC;
      if (r < NRT) RD128(fa[nb][r], a0, (RT0 + r) * 2048);
      else RD128(fa[nb][r], g0, (r - NRT) * 2048);
    } else if (k < 2 * NC + NA + npc) {
      const int i = k - 2 * NC - NA;
      k1_piece(c, lds, i, sb, sa, live, npc == 3);
    }
  };
  auto wait_frags = [&](int buf) {
    asm volatile("s_waitcnt lgkmcnt(0)"
                 : "+v"(fa[buf][0]), "+v"(fa[buf][1]), "+v"(fa[buf][2]), "+v"(fa[buf][3]),
                   "+v"(fb[buf][0][0]), "+v"(fb[buf][0][1]), "+v"(fb[buf][1][0]), "+v"(fb[buf][1][1]), "+v"(fb[buf][2][0]),
                   "+v"(fb[buf][2][1]), "+v"(fb[buf][3][0]), "+v"(fb[buf][3][1])
                 :: "memory");
#pragma unroll
    for (int r = 4; r < NA; ++r) asm volatile("" : "+v"(fa[buf][r]) :: "memory");
#pragma unroll
    for (int q = 4; q < NC; ++q) asm volatile("" : "+v"(fb[buf][q][0]), "+v"(fb[buf][q][1]) :: "memory");
  };
  WAITV(8);
  __builtin_amdgcn_s_barrier();
  STAMP(1);
  bases(0, 0, 0);
#pragma unroll
  for (int k = 0; k < 2 * NC + NA; ++k) memop(0, k, 0, 0, 0, true, true);
  unsigned sw = 0;
  unsigned sa = 0;
  auto step = [&](int b, bool first, bool live, unsigned san, bool second = false) {
    const unsigned sn = (sw == 4 * BSLOT) ? 0u : sw + BSLOT;
    const unsigned sap = (sa == 0) ? 2 * ASLOT : sa - ASLOT;
    const unsigned sp = sw >= 2 * BSLOT ? sw - 2 * BSLOT : sw + 3 * BSLOT;
    bases(b ^ 1, sn, b ? san : sa);
    if (first) WAITV(5); else if (second) WAITV(3); else if (b == 0) WAITV(5); else WAITV(3);
    wait_frags(b);
    __builtin_amdgcn_s_barrier();
    const int npc = b ? 5 : 3;
    auto mo = [&](int k) {
      if (first && k >= 2 * NC + NA) {
        const int i = k - 2 * NC - NA;
        if (i < 3) k1_piece(c, lds, i, 3 * BSLOT, 0, true, true);
        else if (i < 6 - NPF0) k1_pf(c, lds);
      } else {
        if (first) memop(b ^ 1, k, npc, sp, sap, live, live);
        else if ((k % GM) == GM - 1 && (k / GM) < npc) memop(b ^ 1, 2 * NC + NA + (k / GM), npc, sp, sap, live, live);
        else { const int kr = k - ((k / GM) < npc ? (k / GM) : npc); if (kr < 2 * NC + NA) memop(b ^ 1, kr, npc, sp, sap, live, live); }
      }
    };
    int k = 0;
#pragma unroll
    for (int ci = 0; ci < NC; ++ci)
#pragma unroll
      for (int r = 0; r < NRT; ++r) {
        acc[r][ci] = MFMA(cat2(fb[b][ci][0], fb[b][ci][1]), asbf(fa[b][r]), acc[r][ci]);
        mo(k); ++k;
        __builtin_amdgcn_sched_barrier(0);
      }
#pragma unroll
    for (int r = 0; r < NG; ++r) {
      gacc[r] = MFMA(asbf(fa[b][NRT + r]), asbf(fa[b][NRT + r]), gacc[r]);
      mo(k); ++k;
      __builtin_amdgcn_sched_barrier(0);
    }
#pragma unroll
    for (int kk = NC * NRT + NG; kk < 2 * NC + NA + 14; ++kk) mo(kk);
    sw = sn;
  };
  step(0, true, true, ASLOT);
  step(1, false, true, ASLOT, true);
  sa = ASLOT;
#pragma unroll 1
  for (int s = 2; s < 20; s += 2) {
    const unsigned san = (sa == 2 * ASLOT) ? 0u : sa + ASLOT;
    step(0, false, s < 17, san);
    step(1, false, s + 1 < 17, san);
    sa = san;
  }
  asm volatile("s_waitcnt vmcnt(0) lgkmcnt(0)" ::: "memory");
  __builtin_amdgcn_s_barrier();
  STAMP(2);
  float* st_rs = (float*)(lds + STAT_OFF);
  float* st_sq = st_rs + 144;
  float* st_cs = st_sq + 144;
  if constexpr (NG > 0) {
    const int rr = lane & 3;
    if (((lane & 15) >> 2) == (lane >> 4)) {
#pragma unroll
      for (int r = 0; r < NG; ++r)
        st_sq[16 * (gt0 + r) + (lane & 15)] = rr == 0 ? gacc[r][0] : rr == 1 ? gacc[r][1] : rr == 2 ? gacc[r][2] : gacc[r][3];
    }
    if (lst == 3 && (lane >> 4) == 2) {
#pragma unroll
      for (int r = 0; r < NRT; ++r) st_rs[16 * (RT0 + r) + (lane & 15)] = acc[r][1][0];
    }
  }
  int ctile[NC];
  ctile[0] = 2 * lst; ctile[1] = 2 * lst + 1;
#pragma unroll
  for (int p = 0; p < NP; ++p) { ctile[2 + p] = 8 + p0 + p; ctile[2 + NP + p] = 14 + p0 + p; }
  if constexpr (MH == 0) {
    if ((lane & 15) == 0) {
#pragma unroll
      for (int ci = 0; ci < NC; ++ci) *(f4*)(st_cs + 16 * ctile[ci] + 4 * (lane >> 4)) = acc[0][ci];
    }
  }
  LBAR();
  f4 cs[NC];
  bf16_t* hp[NP];
#pragma unroll
  for (int p = 0; p < NP; ++p) hp[p] = A2 + a2_off(r0 + (lane & 15), 640 + 96 * cg + 16 * (p0 + p) + 4 * (lane >> 4));
#pragma unroll
  for (int ci = 0; ci < NC; ++ci) cs[ci] = *(const f4*)(st_cs + 16 * ctile[ci] + 4 * (lane >> 4));
  float nm[NRT], sd[NRT], rinv[NRT];
#pragma unroll
  for (int r = 0; r < NRT; ++r) {
    const int row = 16 * (RT0 + r) + (lane & 15);
    const float mean = st_rs[row] * (1.0f / 640.0f);
    const float var = fmaxf(st_sq[row] * (1.0f / 640.0f) - mean * mean, 0.f);
    nm[r] = -bfr(mean);
    sd[r] = bfr(__builtin_amdgcn_sqrtf(bfr(bfr(var) + 1e-5f)));
    rinv[r] = __builtin_amdgcn_rcpf(sd[r]);
  }
#pragma unroll
  for (int r = 0; r < NRT; ++r) {
    const int row = 16 * (RT0 + r) + (lane & 15);
    const f4 nm4 = {nm[r], nm[r], nm[r], nm[r]};
#pragma unroll
    for (int ci = 0; ci < 2; ++ci) {
      f4 y = __builtin_elementwise_fma(cs[ci], nm4, acc[r][ci]) * rinv[r];
      u2 o; o[0] = pk2(y[0], y[1]); o[1] = pk2(y[2], y[3]);
      *(u2*)(lds + row * IMG_STRIDE + (16 * ctile[ci] + 4 * (lane >> 4)) * 2) = o;
    }
  }
  constexpr int R1 = MH == 0 ? 1 : 0;
#pragma unroll
  for (int r = R1; r < NRT; ++r) {
    const f4 nm4 = {nm[r], nm[r], nm[r], nm[r]};
    const float ga = -1.4426950408889634f * rinv[r];
#pragma unroll
    for (int p = 0; p < NP; ++p) {
      acc[r][2 + p] = __builtin_elementwise_fma(cs[2 + p], nm4, acc[r][2 + p]) * ga;
      acc[r][2 + NP + p] = __builtin_elementwise_fma(cs[2 + NP + p], nm4, acc[r][2 + NP + p]);
    }
  }
#pragma unroll
  for (int r = R1; r < NRT; ++r)
#pragma unroll
    for (int p = 0; p < NP; ++p)
#pragma unroll
      for (int e = 0; e < 4; ++e) acc[r][2 + p][e] = __builtin_amdgcn_exp2f(acc[r][2 + p][e]);
#pragma unroll
  for (int r = R1; r < NRT; ++r) {
    const f4 sd4 = {sd[r], sd[r], sd[r], sd[r]};
#pragma unroll
    for (int p = 0; p < NP; ++p) acc[r][2 + p] = __builtin_elementwise_fma(acc[r][2 + p], sd4, sd4);
  }
#pragma unroll
  for (int r = R1; r < NRT; ++r)
#pragma unroll
    for (int p = 0; p < NP; ++p)
#pragma unroll
      for (int e = 0; e < 4; ++e) acc[r][2 + p][e] = __builtin_amdgcn_rcpf(acc[r][2 + p][e]);
#pragma unroll
  for (int r = R1; r < NRT; ++r) {
    const int rt = RT0 + r - 1;
#pragma unroll
    for (int p = 0; p < NP; ++p) {
      f4 d = acc[r][2 + p] * acc[r][2 + NP + p];
      u2 o; o[0] = pk2(d[0], d[1]); o[1] = pk2(d[2], d[3]);
      ST8O(hp[p] + (rt >> 2) * (68 * 4 * 512), o, (rt & 3) * 1024);
    }
  }
}

DEVI float xor1(float v) { return __builtin_bit_cast(float, __builtin_amdgcn_mov_dpp(__builtin_bit_cast(int, v), 0xB1, 0xf, 0xf, true)); }

__global__ __launch_bounds__(512, 2) void k1_kernel(const bf16_t* __restrict__ x, const bf16_t* __restrict__ wqkv,
                                                    const bf16_t* __restrict__ wg, const bf16_t* __restrict__ wu,
                                                    const bf16_t* __restrict__ wo, const bf16_t* __restrict__ wd,
                                                    bf16_t* __restrict__ A2, bf16_t* __restrict__ W2f) {
  __shared__ __attribute__((aligned(16))) char lds[DUMMY_OFF + 1024];
  const int tid = threadIdx.x, lane = tid & 63;
  const int w = __builtin_amdgcn_readfirstlane(tid >> 6);
  const int bid = blockIdx.x, xcd = bid & 7, idx = bid >> 3;
  const int rb = 8 * (xcd & 1) + (idx & 7), cg = 4 * (xcd >> 1) + (idx >> 3);
  const int r0 = rb * 128;
  unsigned long long* dbgp = (unsigned long long*)(A2 + 8 * 1024 * 1024);
  STAMP(0);
  K1Ctx c;
  {
    const int gw = bid * 8 + w;
    const char* pb = (const char*)g_ones; unsigned per = 0; int id = 0;
    if (gw < 400) { pb = (const char*)wqkv; per = 400; id = gw; }
    else if (gw < 720) { pb = (const char*)wg; per = 320; id = gw - 400; }
    else if (gw < 1040) { pb = (const char*)wu; per = 320; id = gw - 720; }
    else if (gw < 1466) { pb = (const char*)x; per = 426; id = gw - 1040; }
    else if (gw < 1599) { pb = (const char*)wo; per = 133; id = gw - 1466; }
    else if (gw < 1919) { pb = (const char*)wd; per = 320; id = gw - 1599; }
    c.pf = per ? pb + id * 1024 + lane * 16 : pb;
    c.pfs = per * 1024;
  }
#pragma unroll
  for (int i = 0; i < NPF0; ++i) k1_pf(c, lds);
  __builtin_amdgcn_sched_barrier(0);
  {
#pragma unroll
    for (int i = 0; i < 5; ++i) {
      const char* src = (const char*)g_ones; unsigned str = 0, kind = 2, dst = DUMMY_OFF;
      const int pb = w + 8 * i;
      const int pa = i == 2 ? 12 + w : w + 8 * (i - 3);
      if (i < 3 && pb < 20) {
        const unsigned ap = pb * 1024 + lane * 16;
        const unsigned k = ap / 640;
        const unsigned al = ap ^ ((((k >> 1) & 1) | (((k >> 3) & 1) << 1)) << 5);
        const unsigned ch = (al - k * 640) >> 4;
        kind = 0; dst = BOFF + pb * 1024;
        if (ch < 15) {
          src = (const char*)(wqkv + k * 1920 + ((ch * 13) >> 6) * 600 + 40 * cg + 8 * ch); str = 32u * 1920 * 2;
        } else if (ch > 15) {
          src = (const char*)((ch < 28 ? wg + 8 * (ch - 16) : wu + 8 * (ch - 28)) + k * 1536 + 96 * cg); str = 32u * 1536 * 2;
        }
      } else if (i >= 3 || (i == 2 && w < 6)) {
        const int lr = 8 * pa + (lane >> 3);
        int grow = r0 - 16 + lr;
        grow = grow < 0 ? 0 : grow;
        kind = 1; dst = pa * 1024;
        if (lr != 0) { src = (const char*)(x + grow * 640 + (((lane & 7) ^ (lr & 7)) * 8)); str = i == 2 ? 64u : 128u; }
      }
      c.lo[i] = (unsigned)(uint64_t)src; c.hi[i] = (unsigned)((uint64_t)src >> 32); c.str[i] = str;
      c.pdst[i] = dst; c.kind[i] = kind;
    }
  }
  STAMP(5);
#pragma unroll
  for (int s = 0; s < 3; ++s) {
#pragma unroll
    for (int i = 0; i < ((s & 1) ? 3 : 5); ++i) k1_piece(c, lds, i, s * BSLOT, (s >> 1) * ASLOT, true, (s & 1) != 0);
    if (NPF0 + s < 6) k1_pf(c, lds);
    if (s < 2) __builtin_amdgcn_s_barrier();
  }
  STAMP(6);
  {
    const int lst = w < 4 ? w : ((w + 2) & 3);
    if (w < 2) k1_main<0, 2>(c, lds, lane, lst, r0, cg, A2, dbgp);
    else if (w < 4) k1_main<0, 1>(c, lds, lane, lst, r0, cg, A2, dbgp);
    else if (w < 6) k1_main<1, 1>(c, lds, lane, lst, r0, cg, A2, dbgp);
    else k1_main<1, 2>(c, lds, lane, lst, r0, cg, A2, dbgp);
  }
  const int gw = bid * 8 + w;
  unsigned short wv[2][8];
#pragma unroll
  for (int t = 0; t < 2; ++t) {
    const int fi = gw + 2048 * t;
    if (t == 0 || fi < 2720) {
      const int ks = fi / 40, ct = fi - ks * 40;
      const bf16_t* p = (ks < 20 ? wo + ks * 32 * 640 : wd + (ks - 20) * 32 * 640) + (8 * (lane >> 4)) * 640 + 16 * ct + (lane & 15);
#pragma unroll
      for (int e = 0; e < 8; ++e) wv[t][e] = p[e * 640];
    }
  }
  LBAR();
  STAMP(3);
  {
    const int half = tid & 1, pi = tid >> 1, hh = pi >> 7, row = pi & 127, pos = r0 + row;
    const char* pr = lds + (16 + row) * IMG_STRIDE + 40 * hh;
    const char* pw = pr - 8 * half * IMG_STRIDE;
    unsigned qv[10];
#pragma unroll
    for (int i = 0; i < 5; ++i) { u2 t = *(const u2*)(pr + 8 * i); qv[2 * i] = t[0]; qv[2 * i + 1] = t[1]; }
    float lg[4];
#pragma unroll
    for (int ww = 0; ww < 4; ++ww) {
      const char* pk = pw - 2 * ww * IMG_STRIDE + 80;
      float a = 0.f;
#pragma unroll
      for (int i = 0; i < 5; ++i) {
        u2 t = *(const u2*)(pk + 8 * i);
        a = dot2(qv[2 * i], t[0], a); a = dot2(qv[2 * i + 1], t[1], a);
      }
      a = bfr(bfr(a) * 0.22360679774997896f);
      lg[ww] = (pos - 2 * (4 * half + ww) >= 0) ? a : -1e9f;
    }
    float mx = fmaxf(fmaxf(lg[0], lg[1]), fmaxf(lg[2], lg[3]));
    mx = fmaxf(mx, xor1(mx));
    float sum = 0.f;
#pragma unroll
    for (int ww = 0; ww < 4; ++ww) { lg[ww] = __expf(lg[ww] - mx); sum += lg[ww]; }
    sum += xor1(sum);
    const float inv = __builtin_amdgcn_rcpf(sum);
    float o[20];
#pragma unroll
    for (int i = 0; i < 20; ++i) o[i] = 0.f;
#pragma unroll
    for (int wp = 0; wp < 2; ++wp) {
      const unsigned P = pk2(lg[2 * wp] * inv, lg[2 * wp + 1] * inv);
      const char* pv0 = pw - 4 * wp * IMG_STRIDE + 160;
      const char* pv1 = pv0 - 2 * IMG_STRIDE;
#pragma unroll
      for (int i = 0; i < 5; ++i) {
        u2 a = *(const u2*)(pv0 + 8 * i), b = *(const u2*)(pv1 + 8 * i);
#pragma unroll
        for (int e = 0; e < 2; ++e) {
          unsigned lo = __builtin_amdgcn_perm(b[e], a[e], 0x05040100u);
          unsigned hi = __builtin_amdgcn_perm(b[e], a[e], 0x07060302u);
          o[4 * i + 2 * e] = dot2(lo, P, o[4 * i + 2 * e]);
          o[4 * i + 2 * e + 1] = dot2(hi, P, o[4 * i + 2 * e + 1]);
        }
      }
    }
#pragma unroll
    for (int i = 0; i < 20; ++i) o[i] += xor1(o[i]);
    const int kc0 = 40 * cg + 20 * hh;
#pragma unroll
    for (int i = 0; i < 3; ++i) {
      const int pc = half ? 2 + i : i;
      u2 t;
      t[0] = half ? pk2(o[4 * (2 + i)], o[4 * (2 + i) + 1]) : pk2(o[4 * i], o[4 * i + 1]);
      t[1] = half ? pk2(o[4 * (2 + i) + 2], o[4 * (2 + i) + 3]) : pk2(o[4 * i + 2], o[4 * i + 3]);
      ST8(A2 + a2_off(pos, kc0 + 4 * pc), t);
    }
  }
#pragma unroll
  for (int t = 0; t < 2; ++t) {
    const int fi = gw + 2048 * t;
    if (t == 0 || fi < 2720) {
      u4 o;
#pragma unroll
      for (int e = 0; e < 4; ++e) o[e] = (unsigned)wv[t][2 * e] | ((unsigned)wv[t][2 * e + 1] << 16);
      ST16(W2f + ((size_t)fi * 64 + lane) * 8, o);
    }
  }
  STAMP(4);
}

#define PDV 2
constexpr int PD = PDV;

__global__ __launch_bounds__(256, 1) void k2_kernel(const bf16_t* __restrict__ A2, const bf16_t* __restrict__ W2f,
                                                    const bf16_t* __restrict__ x, bf16_t* __restrict__ out) {
  __shared__ __attribute__((aligned(16))) char lds[4 * 15 * 1024];
  const int tid = threadIdx.x, lane = tid & 63;
  const int w = __builtin_amdgcn_readfirstlane(tid >> 6);
  const int bid = blockIdx.x, xcd = bid & 7, idx = bid >> 3;
  const int rb64 = 8 * (xcd & 3) + (idx & 7), cb = 4 * (xcd >> 2) + (idx >> 3);
  unsigned long long* dbgp = (unsigned long long*)((bf16_t*)A2 + 9 * 1024 * 1024);
  STAMP(0);
  f4 acc[4][5];
#pragma unroll
  for (int r = 0; r < 4; ++r)
#pragma unroll
    for (int q = 0; q < 5; ++q) acc[r][q] = f4{0.f, 0.f, 0.f, 0.f};
  const char* ap[4];
#pragma unroll
  for (int r = 0; r < 4; ++r) ap[r] = (const char*)(A2 + ((size_t)((rb64 * 68 + w) * 4 + ((r + w) & 3)) * 64 + lane) * 8);
  const char* bp = (const char*)(W2f + ((size_t)(w * 40 + 5 * cb) * 64 + lane) * 8);
  u4 fa[PD][4], fb[PD][5];
  auto issue = [&](int i) {
    const char* b = bp + (size_t)i * 4 * 40 * 1024;
#pragma unroll
    for (int r = 0; r < 4; ++r)
      asm volatile("global_load_dwordx4 %0, %1, off" : "=v"(fa[i % PD][r]) : "v"(ap[r] + (size_t)i * 4 * 4096));
#pragma unroll
    for (int q = 0; q < 4; ++q)
      asm volatile("global_load_dwordx4 %0, %1, off offset:%2" : "=a"(fb[i % PD][q]) : "v"(b), "n"(q * 1024));
    asm volatile("global_load_dwordx4 %0, %1, off offset:%2" : "=a"(fb[i % PD][4]) : "v"(b + 4096), "n"(0));
  };
  const int grow = rb64 * 64 + 16 * w + (lane & 15), gcol = 80 * cb + 4 * (lane >> 4);
  u2 xv[5];
#pragma unroll
  for (int i = 0; i < PD - 1; ++i) issue(i);
#pragma unroll
  for (int i = 0; i < 17; ++i) {
    const int b = i % PD;
    if (i + PD - 1 < 17) issue(i + PD - 1);
    if (i + PD - 1 == 17) {
      const bf16_t* xp = x + grow * 640 + gcol;
#pragma unroll
      for (int ci = 0; ci < 5; ++ci)
        asm volatile("global_load_dwordx2 %0, %1, off offset:%2" : "=v"(xv[ci]) : "v"(xp), "n"(ci * 32));
    }
    const int rem = i + PD - 1 < 17 ? (PD - 1) * 9 : (16 - i) * 9 + 5;
    asm volatile("s_waitcnt vmcnt(%9)" : "+v"(fa[b][0]), "+v"(fa[b][1]), "+v"(fa[b][2]), "+v"(fa[b][3]),
                 "+a"(fb[b][0]), "+a"(fb[b][1]), "+a"(fb[b][2]), "+a"(fb[b][3]), "+a"(fb[b][4]) : "n"(rem) : "memory");
#pragma unroll
    for (int r = 3; r >= 0; --r)
#pragma unroll
      for (int ci = 0; ci < 5; ++ci) acc[r][ci] = MFMA(asbf(fb[b][ci]), asbf(fa[b][r]), acc[r][ci]);
  }
  STAMP(1);
#pragma unroll
  for (int r = 3; r >= 1; --r) {
    char* d = lds + ((((r + w) & 3) * 3 + r - 1) * 5) * 1024 + lane * 16;
#pragma unroll
    for (int q = 0; q < 5; ++q) *(f4*)(d + q * 1024) = acc[r][q];
  }
  LBAR();
  asm volatile("s_waitcnt vmcnt(0)" : "+v"(xv[0]), "+v"(xv[1]), "+v"(xv[2]), "+v"(xv[3]), "+v"(xv[4]) :: "memory");
  STAMP(2);
#pragma unroll
  for (int q = 0; q < 5; ++q) {
    f4 s = acc[0][q];
#pragma unroll
    for (int t = 0; t < 3; ++t) s += *(const f4*)(lds + ((w * 3 + t) * 5 + q) * 1024 + lane * 16);
    u2 o;
    o[0] = pk2(s[0] + lo16(xv[q][0]), s[1] + hi16(xv[q][0]));
    o[1] = pk2(s[2] + lo16(xv[q][1]), s[3] + hi16(xv[q][1]));
    *(u2*)(out + grow * 640 + gcol + 16 * q) = o;
  }
  STAMP(3);
}

extern "C" void kernel_launch(void* const* d_in, const int* in_sizes, int n_in, void* d_out, int out_size,
                              void* d_ws, size_t ws_size, hipStream_t stream) {
  const bf16_t* x = (const bf16_t*)d_in[0];
  const bf16_t* wqkv = (const bf16_t*)d_in[1];
  const bf16_t* wo = (const bf16_t*)d_in[2];
  const bf16_t* wg = (const bf16_t*)d_in[4];
  const bf16_t* wu = (const bf16_t*)d_in[5];
  const bf16_t* wd = (const bf16_t*)d_in[6];
  bf16_t* A2 = (bf16_t*)d_ws;
  bf16_t* W2f = A2 + 6 * 1024 * 1024;
  k1_kernel<<<256, 512, 0, stream>>>(x, wqkv, wg, wu, wo, wd, A2, W2f);
  k2_kernel<<<256, 256, 0, stream>>>(A2, W2f, x, (bf16_t*)d_out);
}
```

```cpp
#include <hip/hip_runtime.h>
#include <hip/hip_bf16.h>
#include <stdint.h>

typedef __bf16 bf8 __attribute__((ext_vector_type(8)));
typedef __bf16 bf2 __attribute__((ext_vector_type(2)));
typedef float f4 __attribute__((ext_vector_type(4)));
typedef unsigned u4 __attribute__((ext_vector_type(4)));
typedef unsigned u2 __attribute__((ext_vector_type(2)));
typedef unsigned short bf16_t;

#define LAS __attribute__((address_space(3)))
#define GAS __attribute__((address_space(1)))
#define DEVI __device__ __forceinline__

__device__ const __attribute__((aligned(16))) unsigned g_ones[4] = {0x3F803F80u, 0x3F803F80u, 0x3F803F80u, 0x3F803F80u};

DEVI float bfr(float v) { return (float)(__bf16)v; }
DEVI unsigned pk2(float a, float b) { bf2 c; c[0] = (__bf16)a; c[1] = (__bf16)b; return __builtin_bit_cast(unsigned, c); }
DEVI float lo16(unsigned v) { return __builtin_bit_cast(float, v << 16); }
DEVI float hi16(unsigned v) { return __builtin_bit_cast(float, v & 0xffff0000u); }
DEVI float dot2(unsigned a, unsigned b, float c) {
  return __builtin_amdgcn_fdot2_f32_bf16(__builtin_bit_cast(bf2, a), __builtin_bit_cast(bf2, b), c, false);
}
DEVI void glds16(const void* g, unsigned ldsoff, char* lds) {
  __builtin_amdgcn_global_load_lds((const GAS void*)g, (LAS void*)(lds + ldsoff), 16, 0, 0);
}
#define RD128(d, addr, off) asm volatile("ds_read_b128 %0, %1 offset:%2" : "=v"(d) : "v"(addr), "n"(off))
#define RDTR(d, addr, off) asm volatile("ds_read_b64_tr_b16 %0, %1 offset:%2" : "=v"(d) : "v"(addr), "n"(off))
DEVI bf8 cat2(u2 a, u2 b) { u4 r; r[0] = a[0]; r[1] = a[1]; r[2] = b[0]; r[3] = b[1]; return __builtin_bit_cast(bf8, r); }
DEVI bf8 asbf(u4 a) { return __builtin_bit_cast(bf8, a); }
#define MFMA(a, b, c) __builtin_amdgcn_mfma_f32_16x16x32_bf16(a, b, c, 0, 0, 0)
#define WAITV(n) asm volatile("s_waitcnt vmcnt(" #n ")" ::: "memory")

#define ST8(p, v) asm volatile("global_store_dwordx2 %0, %1, off sc0 sc1" :: "v"(p), "v"(v) : "memory")
#define ST8O(p, v, o) asm volatile("global_store_dwordx2 %0, %1, off offset:%2 sc0 sc1" :: "v"(p), "v"(v), "n"(o) : "memory")
#define ST16(p, v) asm volatile("global_store_dwordx4 %0, %1, off sc0 sc1\n\ts_nop 1" :: "v"(p), "v"(v) : "memory")
constexpr int GM = 4;
constexpr int NPF0 = 3;
constexpr int ASLOT = 18432;
constexpr int BSLOT = 20480;
constexpr int BOFF = 3 * ASLOT;
constexpr int DUMMY_OFF = BOFF + 5 * BSLOT;
constexpr int IMG_STRIDE = 264;
constexpr int STAT_OFF = 144 * IMG_STRIDE;

DEVI unsigned a2_off(int grow, int kcol) {
  return (((((unsigned)grow >> 6) * 68 + ((unsigned)kcol >> 5)) * 4 + (((unsigned)grow >> 4) & 3)) * 64 +
          (((((unsigned)kcol >> 3) & 3) << 4) | ((unsigned)grow & 15))) * 8 + ((unsigned)kcol & 7);
}

struct K1Ctx {
  unsigned lo[5], hi[5];
  unsigned str[5];
  unsigned pdst[5];
  unsigned kind[5];
  const char* pf;
  unsigned pfs;
};
DEVI void k1_pf(K1Ctx& c, char* lds) {
  glds16(c.pf, DUMMY_OFF, lds);
  c.pf += c.pfs;
}

DEVI void k1_piece(K1Ctx& c, char* lds, int i, unsigned sb, unsigned sa, bool live, bool oddg) {
  const bool lv = (i == 2 && oddg) ? (live && c.kind[2] == 0) : live;
  const char* p = lv ? (const char*)(((uint64_t)c.hi[i] << 32) | c.lo[i]) : (const char*)g_ones;
  unsigned d;
  if (i < 2) d = sb + c.pdst[i];
  else if (i > 2) d = sa + c.pdst[i];
  else if (oddg) d = c.kind[2] == 0 ? sb + c.pdst[2] : (unsigned)DUMMY_OFF;
  else d = (c.kind[2] == 0 ? sb : c.kind[2] == 1 ? sa : 0u) + c.pdst[2];
  glds16(p, d, lds);
  asm("v_add_co_u32 %0, vcc, %0, %2\n\tv_addc_co_u32 %1, vcc, 0, %1, vcc" : "+v"(c.lo[i]), "+v"(c.hi[i]) : "v"(c.str[i]) : "vcc");
}
#define LBAR() do { asm volatile("s_waitcnt lgkmcnt(0)" ::: "memory"); __builtin_amdgcn_s_barrier(); asm volatile("" ::: "memory"); } while (0)

template <int MH, int NP>
DEVI void k1_main(K1Ctx& c, char* lds, int lane, int lst, int r0, int cg, bf16_t* __restrict__ A2) {
  constexpr int NRT = MH == 0 ? 5 : 4;
  constexpr int RT0 = MH == 0 ? 0 : 5;
  constexpr int NC = 2 + 2 * NP;
  constexpr int NG = NP == 2 ? 0 : (MH == 0 ? 3 : 2);
  constexpr int NA = NRT + NG;
  f4 acc[NRT][NC];
  f4 gacc[NG + 1];
#pragma unroll
  for (int r = 0; r < NRT; ++r)
#pragma unroll
    for (int q = 0; q < NC; ++q) acc[r][q] = f4{0.f, 0.f, 0.f, 0.f};
#pragma unroll
  for (int r = 0; r < NG + 1; ++r) gacc[r] = f4{0.f, 0.f, 0.f, 0.f};
  const unsigned lbase = (unsigned)(uintptr_t)(LAS char*)lds;
  const int ar = lane & 15, ac = lane >> 4;
  const unsigned abase0 = lbase + ar * 128 + ((ac ^ (ar & 7)) * 16);
  const unsigned abase1 = lbase + ar * 128 + (((4 + ac) ^ (ar & 7)) * 16);
  const int gt0 = MH == 0 ? 3 * (lst - 2) : 6 + (lst - 2);
  const int g = lane >> 4, q4 = (lane & 15) >> 2, p4 = lane & 3;
  const int p0 = NP == 2 ? 2 * lst : lst + 2;
  const unsigned hx = (((q4 >> 1) & 1) | ((g & 1) << 1)) << 5;
  auto baddr = [&](int ct) { return lbase + BOFF + ((((8 * g + q4) * 640 + 32 * ct) ^ hx) + 8 * p4); };
  unsigned bb[NC];
  bb[0] = baddr(2 * lst); bb[1] = baddr(2 * lst + 1);
#pragma unroll
  for (int p = 0; p < NP; ++p) { bb[2 + p] = baddr(8 + p0 + p); bb[2 + NP + p] = baddr(14 + p0 + p); }
  u4 fa[2][NA];
  u2 fb[2][NC][2];
  unsigned a0, g0, bc[NC];
  auto bases = [&](int odd, unsigned sb, unsigned sa) {
    a0 = (odd ? abase1 : abase0) + sa; g0 = a0 + gt0 * 2048;
#pragma unroll
    for (int ci = 0; ci < NC; ++ci) bc[ci] = bb[ci] + sb;
  };
  auto memop = [&](int nb, int k, int npc, unsigned sb, unsigned sa, bool live, bool liveA) {
    if (k < 2 * NC) {
      RDTR(fb[nb][k >> 1][k & 1], bc[k >> 1], (k & 1) * 2560);
    } else if (k < 2 * NC + NA) {
      const int r = k - 2 * NC;
      if (r < NRT) RD128(fa[nb][r], a0, (RT0 + r) * 2048);
      else RD128(fa[nb][r], g0, (r - NRT) * 2048);
    } else if (k < 2 * NC + NA + npc) {
      const int i = k - 2 * NC - NA;
      k1_piece(c, lds, i, sb, sa, live, npc == 3);
    }
  };
  auto wait_frags = [&](int buf) {
    asm volatile("s_waitcnt lgkmcnt(0)"
                 : "+v"(fa[buf][0]), "+v"(fa[buf][1]), "+v"(fa[buf][2]), "+v"(fa[buf][3]),
                   "+v"(fb[buf][0][0]), "+v"(fb[buf][0][1]), "+v"(fb[buf][1][0]), "+v"(fb[buf][1][1]), "+v"(fb[buf][2][0]),
                   "+v"(fb[buf][2][1]), "+v"(fb[buf][3][0]), "+v"(fb[buf][3][1])
                 :: "memory");
#pragma unroll
    for (int r = 4; r < NA; ++r) asm volatile("" : "+v"(fa[buf][r]) :: "memory");
#pragma unroll
    for (int q = 4; q < NC; ++q) asm volatile("" : "+v"(fb[buf][q][0]), "+v"(fb[buf][q][1]) :: "memory");
  };
  WAITV(8);
  __builtin_amdgcn_s_barrier();
  bases(0, 0, 0);
#pragma unroll
  for (int k = 0; k < 2 * NC + NA; ++k) memop(0, k, 0, 0, 0, true, true);
  unsigned sw = 0;
  unsigned sa = 0;
  auto step = [&](int b, bool live, unsigned san) {
    const unsigned sn = (sw == 4 * BSLOT) ? 0u : sw + BSLOT;
    const unsigned sap = (sa == 0) ? 2 * ASLOT : sa - ASLOT;
    const unsigned sp = sw >= 2 * BSLOT ? sw - 2 * BSLOT : sw + 3 * BSLOT;
    bases(b ^ 1, sn, b ? san : sa);
    if (b == 0) WAITV(5); else WAITV(3);
    wait_frags(b);
    __builtin_amdgcn_s_barrier();
    const int npc = b ? 5 : 3;
    auto mo = [&](int k) {
      if ((k % GM) == GM - 1 && (k / GM) < npc) memop(b ^ 1, 2 * NC + NA + (k / GM), npc, sp, sap, live, live);
      else { const int kr = k - ((k / GM) < npc ? (k / GM) : npc); if (kr < 2 * NC + NA) memop(b ^ 1, kr, npc, sp, sap, live, live); }
    };
    int k = 0;
#pragma unroll
    for (int ci = 0; ci < NC; ++ci)
#pragma unroll
      for (int r = 0; r < NRT; ++r) {
        acc[r][ci] = MFMA(cat2(fb[b][ci][0], fb[b][ci][1]), asbf(fa[b][r]), acc[r][ci]);
        mo(k); ++k;
        __builtin_amdgcn_sched_barrier(0);
      }
#pragma unroll
    for (int r = 0; r < NG; ++r) {
      gacc[r] = MFMA(asbf(fa[b][NRT + r]), asbf(fa[b][NRT + r]), gacc[r]);
      mo(k); ++k;
      __builtin_amdgcn_sched_barrier(0);
    }
#pragma unroll
    for (int kk = NC * NRT + NG; kk < 2 * NC + NA + 8; ++kk) mo(kk);
    sw = sn;
  };
#pragma unroll 1
  for (int s = 0; s < 20; s += 2) {
    const unsigned san = (sa == 2 * ASLOT) ? 0u : sa + ASLOT;
    step(0, s < 17, san);
    step(1, s + 1 < 17, san);
    sa = san;
  }
  asm volatile("s_waitcnt vmcnt(0) lgkmcnt(0)" ::: "memory");
  __builtin_amdgcn_s_barrier();
  float* st_rs = (float*)(lds + STAT_OFF);
  float* st_sq = st_rs + 144;
  float* st_cs = st_sq + 144;
  if constexpr (NG > 0) {
    const int rr = lane & 3;
    if (((lane & 15) >> 2) == (lane >> 4)) {
#pragma unroll
      for (int r = 0; r < NG; ++r)
        st_sq[16 * (gt0 + r) + (lane & 15)] = rr == 0 ? gacc[r][0] : rr == 1 ? gacc[r][1] : rr == 2 ? gacc[r][2] : gacc[r][3];
    }
    if (lst == 3 && (lane >> 4) == 2) {
#pragma unroll
      for (int r = 0; r < NRT; ++r) st_rs[16 * (RT0 + r) + (lane & 15)] = acc[r][1][0];
    }
  }
  int ctile[NC];
  ctile[0] = 2 * lst; ctile[1] = 2 * lst + 1;
#pragma unroll
  for (int p = 0; p < NP; ++p) { ctile[2 + p] = 8 + p0 + p; ctile[2 + NP + p] = 14 + p0 + p; }
  if constexpr (MH == 0) {
    if ((lane & 15) == 0) {
#pragma unroll
      for (int ci = 0; ci < NC; ++ci) *(f4*)(st_cs + 16 * ctile[ci] + 4 * (lane >> 4)) = acc[0][ci];
    }
  }
  LBAR();
  f4 cs[NC];
  bf16_t* hp[NP];
#pragma unroll
  for (int p = 0; p < NP; ++p) hp[p] = A2 + a2_off(r0 + (lane & 15), 640 + 96 * cg + 16 * (p0 + p) + 4 * (lane >> 4));
#pragma unroll
  for (int ci = 0; ci < NC; ++ci) cs[ci] = *(const f4*)(st_cs + 16 * ctile[ci] + 4 * (lane >> 4));
  float nm[NRT], sd[NRT], rinv[NRT];
#pragma unroll
  for (int r = 0; r < NRT; ++r) {
    const int row = 16 * (RT0 + r) + (lane & 15);
    const float mean = st_rs[row] * (1.0f / 640.0f);
    const float var = fmaxf(st_sq[row] * (1.0f / 640.0f) - mean * mean, 0.f);
    nm[r] = -bfr(mean);
    sd[r] = bfr(__builtin_amdgcn_sqrtf(bfr(bfr(var) + 1e-5f)));
    rinv[r] = __builtin_amdgcn_rcpf(sd[r]);
  }
#pragma unroll
  for (int r = 0; r < NRT; ++r) {
    const int row = 16 * (RT0 + r) + (lane & 15);
    const f4 nm4 = {nm[r], nm[r], nm[r], nm[r]};
#pragma unroll
    for (int ci = 0; ci < 2; ++ci) {
      f4 y = __builtin_elementwise_fma(cs[ci], nm4, acc[r][ci]) * rinv[r];
      u2 o; o[0] = pk2(y[0], y[1]); o[1] = pk2(y[2], y[3]);
      *(u2*)(lds + row * IMG_STRIDE + (16 * ctile[ci] + 4 * (lane >> 4)) * 2) = o;
    }
  }
  constexpr int R1 = MH == 0 ? 1 : 0;
#pragma unroll
  for (int r = R1; r < NRT; ++r) {
    const f4 nm4 = {nm[r], nm[r], nm[r], nm[r]};
    const float ga = -1.4426950408889634f * rinv[r];
#pragma unroll
    for (int p = 0; p < NP; ++p) {
      acc[r][2 + p] = __builtin_elementwise_fma(cs[2 + p], nm4, acc[r][2 + p]) * ga;
      acc[r][2 + NP + p] = __builtin_elementwise_fma(cs[2 + NP + p], nm4, acc[r][2 + NP + p]);
    }
  }
#pragma unroll
  for (int r = R1; r < NRT; ++r)
#pragma unroll
    for (int p = 0; p < NP; ++p)
#pragma unroll
      for (int e = 0; e < 4; ++e) acc[r][2 + p][e] = __builtin_amdgcn_exp2f(acc[r][2 + p][e]);
#pragma unroll
  for (int r = R1; r < NRT; ++r) {
    const f4 sd4 = {sd[r], sd[r], sd[r], sd[r]};
#pragma unroll
    for (int p = 0; p < NP; ++p) acc[r][2 + p] = __builtin_elementwise_fma(acc[r][2 + p], sd4, sd4);
  }
#pragma unroll
  for (int r = R1; r < NRT; ++r)
#pragma unroll
    for (int p = 0; p < NP; ++p)
#pragma unroll
      for (int e = 0; e < 4; ++e) acc[r][2 + p][e] = __builtin_amdgcn_rcpf(acc[r][2 + p][e]);
#pragma unroll
  for (int r = R1; r < NRT; ++r) {
    const int rt = RT0 + r - 1;
#pragma unroll
    for (int p = 0; p < NP; ++p) {
      f4 d = acc[r][2 + p] * acc[r][2 + NP + p];
      u2 o; o[0] = pk2(d[0], d[1]); o[1] = pk2(d[2], d[3]);
      ST8O(hp[p] + (rt >> 2) * (68 * 4 * 512), o, (rt & 3) * 1024);
    }
  }
}

DEVI float xor1(float v) { return __builtin_bit_cast(float, __builtin_amdgcn_mov_dpp(__builtin_bit_cast(int, v), 0xB1, 0xf, 0xf, true)); }

__global__ __launch_bounds__(512, 2) void k1_kernel(const bf16_t* __restrict__ x, const bf16_t* __restrict__ wqkv,
                                                    const bf16_t* __restrict__ wg, const bf16_t* __restrict__ wu,
                                                    const bf16_t* __restrict__ wo, const bf16_t* __restrict__ wd,
                                                    bf16_t* __restrict__ A2, bf16_t* __restrict__ W2f) {
  __shared__ __attribute__((aligned(16))) char lds[DUMMY_OFF + 1024];
  const int tid = threadIdx.x, lane = tid & 63;
  const int w = __builtin_amdgcn_readfirstlane(tid >> 6);
  const int bid = blockIdx.x, xcd = bid & 7, idx = bid >> 3;
  const int rb = 8 * (xcd & 1) + (idx & 7), cg = 4 * (xcd >> 1) + (idx >> 3);
  const int r0 = rb * 128;
  K1Ctx c;
  {
    const int gw = bid * 8 + w;
    const char* pb = (const char*)g_ones; unsigned per = 0; int id = 0;
    if (gw < 400) { pb = (const char*)wqkv; per = 400; id = gw; }
    else if (gw < 720) { pb = (const char*)wg; per = 320; id = gw - 400; }
    else if (gw < 1040) { pb = (const char*)wu; per = 320; id = gw - 720; }
    else if (gw < 1466) { pb = (const char*)x; per = 426; id = gw - 1040; }
    else if (gw < 1599) { pb = (const char*)wo; per = 133; id = gw - 1466; }
    else if (gw < 1919) { pb = (const char*)wd; per = 320; id = gw - 1599; }
    c.pf = per ? pb + id * 1024 + lane * 16 : pb;
    c.pfs = per * 1024;
  }
#pragma unroll
  for (int i = 0; i < NPF0; ++i) k1_pf(c, lds);
  __builtin_amdgcn_sched_barrier(0);
  {
#pragma unroll
    for (int i = 0; i < 5; ++i) {
      const char* src = (const char*)g_ones; unsigned str = 0, kind = 2, dst = DUMMY_OFF;
      const int pb = w + 8 * i;
      const int pa = i == 2 ? 12 + w : w + 8 * (i - 3);
      if (i < 3 && pb < 20) {
        const unsigned ap = pb * 1024 + lane * 16;
        const unsigned k = ap / 640;
        const unsigned al = ap ^ ((((k >> 1) & 1) | (((k >> 3) & 1) << 1)) << 5);
        const unsigned ch = (al - k * 640) >> 4;
        kind = 0; dst = BOFF + pb * 1024;
        if (ch < 15) {
          src = (const char*)(wqkv + k * 1920 + ((ch * 13) >> 6) * 600 + 40 * cg + 8 * ch); str = 32u * 1920 * 2;
        } else if (ch > 15) {
          src = (const char*)((ch < 28 ? wg + 8 * (ch - 16) : wu + 8 * (ch - 28)) + k * 1536 + 96 * cg); str = 32u * 1536 * 2;
        }
      } else if (i >= 3 || (i == 2 && w < 6)) {
        const int lr = 8 * pa + (lane >> 3);
        int grow = r0 - 16 + lr;
        grow = grow < 0 ? 0 : grow;
        kind = 1; dst = pa * 1024;
        if (lr != 0) { src = (const char*)(x + grow * 640 + (((lane & 7) ^ (lr & 7)) * 8)); str = i == 2 ? 64u : 128u; }
      }
      c.lo[i] = (unsigned)(uint64_t)src; c.hi[i] = (unsigned)((uint64_t)src >> 32); c.str[i] = str;
      c.pdst[i] = dst; c.kind[i] = kind;
    }
  }
#pragma unroll
  for (int s = 0; s < 3; ++s) {
#pragma unroll
    for (int i = 0; i < ((s & 1) ? 3 : 5); ++i) k1_piece(c, lds, i, s * BSLOT, (s >> 1) * ASLOT, true, (s & 1) != 0);
    if (NPF0 + s < 6) k1_pf(c, lds);
    if (s < 2) __builtin_amdgcn_s_barrier();
  }
  {
    const int lst = w < 4 ? w : ((w + 2) & 3);
    if (w < 2) k1_main<0, 2>(c, lds, lane, lst, r0, cg, A2);
    else if (w < 4) k1_main<0, 1>(c, lds, lane, lst, r0, cg, A2);
    else if (w < 6) k1_main<1, 1>(c, lds, lane, lst, r0, cg, A2);
    else k1_main<1, 2>(c, lds, lane, lst, r0, cg, A2);
  }
  const int gw = bid * 8 + w;
  unsigned short wv[2][8];
#pragma unroll
  for (int t = 0; t < 2; ++t) {
    const int fi = gw + 2048 * t;
    if (t == 0 || fi < 2720) {
      const int ks = fi / 40, ct = fi - ks * 40;
      const bf16_t* p = (ks < 20 ? wo + ks * 32 * 640 : wd + (ks - 20) * 32 * 640) + (8 * (lane >> 4)) * 640 + 16 * ct + (lane & 15);
#pragma unroll
      for (int e = 0; e < 8; ++e) wv[t][e] = p[e * 640];
    }
  }
  LBAR();
  {
    const int half = tid & 1, pi = tid >> 1, hh = pi >> 7, row = pi & 127, pos = r0 + row;
    const char* pr = lds + (16 + row) * IMG_STRIDE + 40 * hh;
    const char* pw = pr - 8 * half * IMG_STRIDE;
    unsigned qv[10];
#pragma unroll
    for (int i = 0; i < 5; ++i) { u2 t = *(const u2*)(pr + 8 * i); qv[2 * i] = t[0]; qv[2 * i + 1] = t[1]; }
    float lg[4];
#pragma unroll
    for (int ww = 0; ww < 4; ++ww) {
      const char* pk = pw - 2 * ww * IMG_STRIDE + 80;
      float a = 0.f;
#pragma unroll
      for (int i = 0; i < 5; ++i) {
        u2 t = *(const u2*)(pk + 8 * i);
        a = dot2(qv[2 * i], t[0], a); a = dot2(qv[2 * i + 1], t[1], a);
      }
      a = bfr(bfr(a) * 0.22360679774997896f);
      lg[ww] = (pos - 2 * (4 * half + ww) >= 0) ? a : -1e9f;
    }
    float mx = fmaxf(fmaxf(lg[0], lg[1]), fmaxf(lg[2], lg[3]));
    mx = fmaxf(mx, xor1(mx));
    float sum = 0.f;
#pragma unroll
    for (int ww = 0; ww < 4; ++ww) { lg[ww] = __expf(lg[ww] - mx); sum += lg[ww]; }
    sum += xor1(sum);
    const float inv = __builtin_amdgcn_rcpf(sum);
    float o[20];
#pragma unroll
    for (int i = 0; i < 20; ++i) o[i] = 0.f;
#pragma unroll
    for (int wp = 0; wp < 2; ++wp) {
      const unsigned P = pk2(lg[2 * wp] * inv, lg[2 * wp + 1] * inv);
      const char* pv0 = pw - 4 * wp * IMG_STRIDE + 160;
      const char* pv1 = pv0 - 2 * IMG_STRIDE;
#pragma unroll
      for (int i = 0; i < 5; ++i) {
        u2 a = *(const u2*)(pv0 + 8 * i), b = *(const u2*)(pv1 + 8 * i);
#pragma unroll
        for (int e = 0; e < 2; ++e) {
          unsigned lo = __builtin_amdgcn_perm(b[e], a[e], 0x05040100u);
          unsigned hi = __builtin_amdgcn_perm(b[e], a[e], 0x07060302u);
          o[4 * i + 2 * e] = dot2(lo, P, o[4 * i + 2 * e]);
          o[4 * i + 2 * e + 1] = dot2(hi, P, o[4 * i + 2 * e + 1]);
        }
      }
    }
#pragma unroll
    for (int i = 0; i < 20; ++i) o[i] += xor1(o[i]);
    const int kc0 = 40 * cg + 20 * hh;
#pragma unroll
    for (int i = 0; i < 3; ++i) {
      const int pc = half ? 2 + i : i;
      u2 t;
      t[0] = half ? pk2(o[4 * (2 + i)], o[4 * (2 + i) + 1]) : pk2(o[4 * i], o[4 * i + 1]);
      t[1] = half ? pk2(o[4 * (2 + i) + 2], o[4 * (2 + i) + 3]) : pk2(o[4 * i + 2], o[4 * i + 3]);
      ST8(A2 + a2_off(pos, kc0 + 4 * pc), t);
    }
  }
#pragma unroll
  for (int t = 0; t < 2; ++t) {
    const int fi = gw + 2048 * t;
    if (t == 0 || fi < 2720) {
      u4 o;
#pragma unroll
      for (int e = 0; e < 4; ++e) o[e] = (unsigned)wv[t][2 * e] | ((unsigned)wv[t][2 * e + 1] << 16);
      ST16(W2f + ((size_t)fi * 64 + lane) * 8, o);
    }
  }
}

constexpr int PD = 2;

__global__ __launch_bounds__(256, 1) void k2_kernel(const bf16_t* __restrict__ A2, const bf16_t* __restrict__ W2f,
                                                    const bf16_t* __restrict__ x, bf16_t* __restrict__ out) {
  __shared__ __attribute__((aligned(16))) char lds[4 * 15 * 1024];
  const int tid = threadIdx.x, lane = tid & 63;
  const int w = __builtin_amdgcn_readfirstlane(tid >> 6);
  const int bid = blockIdx.x, xcd = bid & 7, idx = bid >> 3;
  const int rb64 = 8 * (xcd & 3) + (idx & 7), cb = 4 * (xcd >> 2) + (idx >> 3);
  f4 acc[4][5];
#pragma unroll
  for (int r = 0; r < 4; ++r)
#pragma unroll
    for (int q = 0; q < 5; ++q) acc[r][q] = f4{0.f, 0.f, 0.f, 0.f};
  const char* ap[4];
#pragma unroll
  for (int r = 0; r < 4; ++r) ap[r] = (const char*)(A2 + ((size_t)((rb64 * 68 + w) * 4 + ((r + w) & 3)) * 64 + lane) * 8);
  const char* bp = (const char*)(W2f + ((size_t)(w * 40 + 5 * cb) * 64 + lane) * 8);
  u4 fa[PD][4], fb[PD][5];
  auto issue = [&](int i) {
    const char* b = bp + (size_t)i * 4 * 40 * 1024;
#pragma unroll
    for (int r = 0; r < 4; ++r)
      asm volatile("global_load_dwordx4 %0, %1, off" : "=v"(fa[i % PD][r]) : "v"(ap[r] + (size_t)i * 4 * 4096));
#pragma unroll
    for (int q = 0; q < 4; ++q)
      asm volatile("global_load_dwordx4 %0, %1, off offset:%2" : "=a"(fb[i % PD][q]) : "v"(b), "n"(q * 1024));
    asm volatile("global_load_dwordx4 %0, %1, off offset:%2" : "=a"(fb[i % PD][4]) : "v"(b + 4096), "n"(0));
  };
  const int grow = rb64 * 64 + 16 * w + (lane & 15), gcol = 80 * cb + 4 * (lane >> 4);
  u2 xv[5];
#pragma unroll
  for (int i = 0; i < PD - 1; ++i) issue(i);
#pragma unroll
  for (int i = 0; i < 17; ++i) {
    const int b = i % PD;
    if (i + PD - 1 < 17) issue(i + PD - 1);
    if (i + PD - 1 == 17) {
      const bf16_t* xp = x + grow * 640 + gcol;
#pragma unroll
      for (int ci = 0; ci < 5; ++ci)
        asm volatile("global_load_dwordx2 %0, %1, off offset:%2" : "=v"(xv[ci]) : "v"(xp), "n"(ci * 32));
    }
    const int rem = i + PD - 1 < 17 ? (PD - 1) * 9 : (16 - i) * 9 + 5;
    asm volatile("s_waitcnt vmcnt(%9)" : "+v"(fa[b][0]), "+v"(fa[b][1]), "+v"(fa[b][2]), "+v"(fa[b][3]),
                 "+a"(fb[b][0]), "+a"(fb[b][1]), "+a"(fb[b][2]), "+a"(fb[b][3]), "+a"(fb[b][4]) : "n"(rem) : "memory");
#pragma unroll
    for (int r = 3; r >= 0; --r)
#pragma unroll
      for (int ci = 0; ci < 5; ++ci) acc[r][ci] = MFMA(asbf(fb[b][ci]), asbf(fa[b][r]), acc[r][ci]);
  }
#pragma unroll
  for (int r = 3; r >= 1; --r) {
    char* d = lds + ((((r + w) & 3) * 3 + r - 1) * 5) * 1024 + lane * 16;
#pragma unroll
    for (int q = 0; q < 5; ++q) *(f4*)(d + q * 1024) = acc[r][q];
  }
  LBAR();
  asm volatile("s_waitcnt vmcnt(0)" : "+v"(xv[0]), "+v"(xv[1]), "+v"(xv[2]), "+v"(xv[3]), "+v"(xv[4]) :: "memory");
#pragma unroll
  for (int q = 0; q < 5; ++q) {
    f4 s = acc[0][q];
#pragma unroll
    for (int t = 0; t < 3; ++t) s += *(const f4*)(lds + ((w * 3 + t) * 5 + q) * 1024 + lane * 16);
    u2 o;
    o[0] = pk2(s[0] + lo16(xv[q][0]), s[1] + hi16(xv[q][0]));
    o[1] = pk2(s[2] + lo16(xv[q][1]), s[3] + hi16(xv[q][1]));
    *(u2*)(out + grow * 640 + gcol + 16 * q) = o;
  }
}

extern "C" void kernel_launch(void* const* d_in, const int* in_sizes, int n_in, void* d_out, int out_size,
                              void* d_ws, size_t ws_size, hipStream_t stream) {
  const bf16_t* x = (const bf16_t*)d_in[0];
  const bf16_t* wqkv = (const bf16_t*)d_in[1];
  const bf16_t* wo = (const bf16_t*)d_in[2];
  const bf16_t* wg = (const bf16_t*)d_in[4];
  const bf16_t* wu = (const bf16_t*)d_in[5];
  const bf16_t* wd = (const bf16_t*)d_in[6];
  bf16_t* A2 = (bf16_t*)d_ws;
  bf16_t* W2f = A2 + 6 * 1024 * 1024;
  k1_kernel<<<256, 512, 0, stream>>>(x, wqkv, wg, wu, wo, wd, A2, W2f);
  k2_kernel<<<256, 256, 0, stream>>>(A2, W2f, x, (bf16_t*)d_out);
}
```

```cpp
#include <hip/hip_runtime.h>
#include <hip/hip_bf16.h>
#include <stdint.h>

typedef __bf16 bf8 __attribute__((ext_vector_type(8)));
typedef __bf16 bf2 __attribute__((ext_vector_type(2)));
typedef float f4 __attribute__((ext_vector_type(4)));
typedef unsigned u4 __attribute__((ext_vector_type(4)));
typedef unsigned u2 __attribute__((ext_vector_type(2)));
typedef unsigned short bf16_t;

#define LAS __attribute__((address_space(3)))
#define GAS __attribute__((address_space(1)))
#define DEVI __device__ __forceinline__

__device__ const __attribute__((aligned(16))) unsigned g_ones[4] = {0x3F803F80u, 0x3F803F80u, 0x3F803F80u, 0x3F803F80u};

DEVI float bfr(float v) { return (float)(__bf16)v; }
DEVI unsigned pk2(float a, float b) { bf2 c; c[0] = (__bf16)a; c[1] = (__bf16)b; return __builtin_bit_cast(unsigned, c); }
DEVI float lo16(unsigned v) { return __builtin_bit_cast(float, v << 16); }
DEVI float hi16(unsigned v) { return __builtin_bit_cast(float, v & 0xffff0000u); }
DEVI float dot2(unsigned a, unsigned b, float c) {
  return __builtin_amdgcn_fdot2_f32_bf16(__builtin_bit_cast(bf2, a), __builtin_bit_cast(bf2, b), c, false);
}
DEVI void glds16(const void* g, unsigned ldsoff, char* lds) {
  __builtin_amdgcn_global_load_lds((const GAS void*)g, (LAS void*)(lds + ldsoff), 16, 0, 0);
}
#define RD128(d, addr, off) asm volatile("ds_read_b128 %0, %1 offset:%2" : "=v"(d) : "v"(addr), "n"(off))
#define RDTR(d, addr, off) asm volatile("ds_read_b64_tr_b16 %0, %1 offset:%2" : "=v"(d) : "v"(addr), "n"(off))
DEVI bf8 cat2(u2 a, u2 b) { u4 r; r[0] = a[0]; r[1] = a[1]; r[2] = b[0]; r[3] = b[1]; return __builtin_bit_cast(bf8, r); }
DEVI bf8 asbf(u4 a) { return __builtin_bit_cast(bf8, a); }
#define MFMA(a, b, c) __builtin_amdgcn_mfma_f32_16x16x32_bf16(a, b, c, 0, 0, 0)
#define WAITV(n) asm volatile("s_waitcnt vmcnt(" #n ")" ::: "memory")

#define ST8(p, v) asm volatile("global_store_dwordx2 %0, %1, off sc0 sc1" :: "v"(p), "v"(v) : "memory")
#define ST8O(p, v, o) asm volatile("global_store_dwordx2 %0, %1, off offset:%2 sc0 sc1" :: "v"(p), "v"(v), "n"(o) : "memory")
#define ST16(p, v) asm volatile("global_store_dwordx4 %0, %1, off sc0 sc1\n\ts_nop 1" :: "v"(p), "v"(v) : "memory")
constexpr int GM = 4;
constexpr int NPF0 = 3;
constexpr int ASLOT = 18432;
constexpr int BSLOT = 20480;
constexpr int BOFF = 3 * ASLOT;
constexpr int DUMMY_OFF = BOFF + 5 * BSLOT;
constexpr int IMG_STRIDE = 264;
constexpr int STAT_OFF = 144 * IMG_STRIDE;

DEVI unsigned a2_off(int grow, int kcol) {
  return (((((unsigned)grow >> 6) * 68 + ((unsigned)kcol >> 5)) * 4 + (((unsigned)grow >> 4) & 3)) * 64 +
          (((((unsigned)kcol >> 3) & 3) << 4) | ((unsigned)grow & 15))) * 8 + ((unsigned)kcol & 7);
}

struct K1Ctx {
  unsigned lo[5], hi[5];
  unsigned str[5];
  unsigned pdst[5];
  unsigned kind[5];
  const char* pf;
  unsigned pfs;
};
DEVI void k1_pf(K1Ctx& c, char* lds) {
  glds16(c.pf, DUMMY_OFF, lds);
  c.pf += c.pfs;
}

DEVI void k1_piece(K1Ctx& c, char* lds, int i, unsigned sb, unsigned sa, bool live, bool oddg) {
  const bool lv = (i == 2 && oddg) ? (live && c.kind[2] == 0) : live;
  const char* p = lv ? (const char*)(((uint64_t)c.hi[i] << 32) | c.lo[i]) : (const char*)g_ones;
  unsigned d;
  if (i < 2) d = sb + c.pdst[i];
  else if (i > 2) d = sa + c.pdst[i];
  else if (oddg) d = c.kind[2] == 0 ? sb + c.pdst[2] : (unsigned)DUMMY_OFF;
  else d = (c.kind[2] == 0 ? sb : c.kind[2] == 1 ? sa : 0u) + c.pdst[2];
  glds16(p, d, lds);
  asm("v_add_co_u32 %0, vcc, %0, %2\n\tv_addc_co_u32 %1, vcc, 0, %1, vcc" : "+v"(c.lo[i]), "+v"(c.hi[i]) : "v"(c.str[i]) : "vcc");
}
#define LBAR() do { asm volatile("s_waitcnt lgkmcnt(0)" ::: "memory"); __builtin_amdgcn_s_barrier(); asm volatile("" ::: "memory"); } while (0)

template <int MH, int NP>
DEVI void k1_main(K1Ctx& c, char* lds, int lane, int lst, int r0, int cg, bf16_t* __restrict__ A2) {
  constexpr int NRT = MH == 0 ? 5 : 4;
  constexpr int RT0 = MH == 0 ? 0 : 5;
  constexpr int NC = 2 + 2 * NP;
  constexpr int NG = NP == 2 ? 0 : (MH == 0 ? 3 : 2);
  constexpr int NA = NRT + NG;
  f4 acc[NRT][NC];
  f4 gacc[NG + 1];
#pragma unroll
  for (int r = 0; r < NRT; ++r)
#pragma unroll
    for (int q = 0; q < NC; ++q) acc[r][q] = f4{0.f, 0.f, 0.f, 0.f};
#pragma unroll
  for (int r = 0; r < NG + 1; ++r) gacc[r] = f4{0.f, 0.f, 0.f, 0.f};
  const unsigned lbase = (unsigned)(uintptr_t)(LAS char*)lds;
  const int ar = lane & 15, ac = lane >> 4;
  const unsigned abase0 = lbase + ar * 128 + ((ac ^ (ar & 7)) * 16);
  const unsigned abase1 = lbase + ar * 128 + (((4 + ac) ^ (ar & 7)) * 16);
  const int gt0 = MH == 0 ? 3 * (lst - 2) : 6 + (lst - 2);
  const int g = lane >> 4, q4 = (lane & 15) >> 2, p4 = lane & 3;
  const int p0 = NP == 2 ? 2 * lst : lst + 2;
  const unsigned hx = (((q4 >> 1) & 1) | ((g & 1) << 1)) << 5;
  auto baddr = [&](int ct) { return lbase + BOFF + ((((8 * g + q4) * 640 + 32 * ct) ^ hx) + 8 * p4); };
  unsigned bb[NC];
  bb[0] = baddr(2 * lst); bb[1] = baddr(2 * lst + 1);
#pragma unroll
  for (int p = 0; p < NP; ++p) { bb[2 + p] = baddr(8 + p0 + p); bb[2 + NP + p] = baddr(14 + p0 + p); }
  u4 fa[2][NA];
  u2 fb[2][NC][2];
  unsigned a0, g0, bc[NC];
  auto bases = [&](int odd, unsigned sb, unsigned sa) {
    a0 = (odd ? abase1 : abase0) + sa; g0 = a0 + gt0 * 2048;
#pragma unroll
    for (int ci = 0; ci < NC; ++ci) bc[ci] = bb[ci] + sb;
  };
  auto memop = [&](int nb, int k, int npc, unsigned sb, unsigned sa, bool live, bool liveA) {
    if (k < 2 * NC) {
      RDTR(fb[nb][k >> 1][k & 1], bc[k >> 1], (k & 1) * 2560);
    } else if (k < 2 * NC + NA) {
      const int r = k - 2 * NC;
      if (r < NRT) RD128(fa[nb][r], a0, (RT0 + r) * 2048);
      else RD128(fa[nb][r], g0, (r - NRT) * 2048);
    } else if (k < 2 * NC + NA + npc) {
      const int i = k - 2 * NC - NA;
      k1_piece(c, lds, i, sb, sa, live, npc == 3);
    }
  };
  auto wait_frags = [&](int buf) {
    asm volatile("s_waitcnt lgkmcnt(0)"
                 : "+v"(fa[buf][0]), "+v"(fa[buf][1]), "+v"(fa[buf][2]), "+v"(fa[buf][3]),
                   "+v"(fb[buf][0][0]), "+v"(fb[buf][0][1]), "+v"(fb[buf][1][0]), "+v"(fb[buf][1][1]), "+v"(fb[buf][2][0]),
                   "+v"(fb[buf][2][1]), "+v"(fb[buf][3][0]), "+v"(fb[buf][3][1])
                 :: "memory");
#pragma unroll
    for (int r = 4; r < NA; ++r) asm volatile("" : "+v"(fa[buf][r]) :: "memory");
#pragma unroll
    for (int q = 4; q < NC; ++q) asm volatile("" : "+v"(fb[buf][q][0]), "+v"(fb[buf][q][1]) :: "memory");
  };
  WAITV(8);
  __builtin_amdgcn_s_barrier();
  bases(0, 0, 0);
#pragma unroll
  for (int k = 0; k < 2 * NC + NA; ++k) memop(0, k, 0, 0, 0, true, true);
  unsigned sw = 0;
  unsigned sa = 0;
  auto step = [&](int b, bool live, unsigned san) {
    const unsigned sn = (sw == 4 * BSLOT) ? 0u : sw + BSLOT;
    const unsigned sap = (sa == 0) ? 2 * ASLOT : sa - ASLOT;
    const unsigned sp = sw >= 2 * BSLOT ? sw - 2 * BSLOT : sw + 3 * BSLOT;
    bases(b ^ 1, sn, b ? san : sa);
    if (b == 0) WAITV(5); else WAITV(3);
    wait_frags(b);
    __builtin_amdgcn_s_barrier();
    const int npc = b ? 5 : 3;
    auto mo = [&](int k) {
      if ((k % GM) == GM - 1 && (k / GM) < npc) memop(b ^ 1, 2 * NC + NA + (k / GM), npc, sp, sap, live, live);
      else { const int kr = k - ((k / GM) < npc ? (k / GM) : npc); if (kr < 2 * NC + NA) memop(b ^ 1, kr, npc, sp, sap, live, live); }
    };
    int k = 0;
#pragma unroll
    for (int ci = 0; ci < NC; ++ci)
#pragma unroll
      for (int r = 0; r < NRT; ++r) {
        acc[r][ci] = MFMA(cat2(fb[b][ci][0], fb[b][ci][1]), asbf(fa[b][r]), acc[r][ci]);
        mo(k); ++k;
        __builtin_amdgcn_sched_barrier(0);
      }
#pragma unroll
    for (int r = 0; r < NG; ++r) {
      gacc[r] = MFMA(asbf(fa[b][NRT + r]), asbf(fa[b][NRT + r]), gacc[r]);
      mo(k); ++k;
      __builtin_amdgcn_sched_barrier(0);
    }
#pragma unroll
    for (int kk = NC * NRT + NG; kk < 2 * NC + NA + 8; ++kk) mo(kk);
    sw = sn;
  };
#pragma unroll 1
  for (int s = 0; s < 20; s += 2) {
    const unsigned san = (sa == 2 * ASLOT) ? 0u : sa + ASLOT;
    step(0, s < 17, san);
    step(1, s + 1 < 17, san);
    sa = san;
  }
  asm volatile("s_waitcnt vmcnt(0) lgkmcnt(0)" ::: "memory");
  __builtin_amdgcn_s_barrier();
  float* st_rs = (float*)(lds + STAT_OFF);
  float* st_sq = st_rs + 144;
  float* st_cs = st_sq + 144;
  if constexpr (NG > 0) {
    const int rr = lane & 3;
    if (((lane & 15) >> 2) == (lane >> 4)) {
#pragma unroll
      for (int r = 0; r < NG; ++r)
        st_sq[16 * (gt0 + r) + (lane & 15)] = rr == 0 ? gacc[r][0] : rr == 1 ? gacc[r][1] : rr == 2 ? gacc[r][2] : gacc[r][3];
    }
    if (lst == 3 && (lane >> 4) == 2) {
#pragma unroll
      for (int r = 0; r < NRT; ++r) st_rs[16 * (RT0 + r) + (lane & 15)] = acc[r][1][0];
    }
  }
  int ctile[NC];
  ctile[0] = 2 * lst; ctile[1] = 2 * lst + 1;
#pragma unroll
  for (int p = 0; p < NP; ++p) { ctile[2 + p] = 8 + p0 + p; ctile[2 + NP + p] = 14 + p0 + p; }
  if constexpr (MH == 0) {
    if ((lane & 15) == 0) {
#pragma unroll
      for (int ci = 0; ci < NC; ++ci) *(f4*)(st_cs + 16 * ctile[ci] + 4 * (lane >> 4)) = acc[0][ci];
    }
  }
  LBAR();
  f4 cs[NC];
  bf16_t* hp[NP];
#pragma unroll
  for (int p = 0; p < NP; ++p) hp[p] = A2 + a2_off(r0 + (lane & 15), 640 + 96 * cg + 16 * (p0 + p) + 4 * (lane >> 4));
#pragma unroll
  for (int ci = 0; ci < NC; ++ci) cs[ci] = *(const f4*)(st_cs + 16 * ctile[ci] + 4 * (lane >> 4));
  float nm[NRT], sd[NRT], rinv[NRT];
#pragma unroll
  for (int r = 0; r < NRT; ++r) {
    const int row = 16 * (RT0 + r) + (lane & 15);
    const float mean = st_rs[row] * (1.0f / 640.0f);
    const float var = fmaxf(st_sq[row] * (1.0f / 640.0f) - mean * mean, 0.f);
    nm[r] = -bfr(mean);
    sd[r] = bfr(__builtin_amdgcn_sqrtf(bfr(bfr(var) + 1e-5f)));
    rinv[r] = __builtin_amdgcn_rcpf(sd[r]);
  }
#pragma unroll
  for (int r = 0; r < NRT; ++r) {
    const int row = 16 * (RT0 + r) + (lane & 15);
    const f4 nm4 = {nm[r], nm[r], nm[r], nm[r]};
#pragma unroll
    for (int ci = 0; ci < 2; ++ci) {
      f4 y = __builtin_elementwise_fma(cs[ci], nm4, acc[r][ci]) * rinv[r];
      u2 o; o[0] = pk2(y[0], y[1]); o[1] = pk2(y[2], y[3]);
      *(u2*)(lds + row * IMG_STRIDE + (16 * ctile[ci] + 4 * (lane >> 4)) * 2) = o;
    }
  }
  constexpr int R1 = MH == 0 ? 1 : 0;
#pragma unroll
  for (int r = R1; r < NRT; ++r) {
    const f4 nm4 = {nm[r], nm[r], nm[r], nm[r]};
    const float ga = -1.4426950408889634f * rinv[r];
#pragma unroll
    for (int p = 0; p < NP; ++p) {
      acc[r][2 + p] = __builtin_elementwise_fma(cs[2 + p], nm4, acc[r][2 + p]) * ga;
      acc[r][2 + NP + p] = __builtin_elementwise_fma(cs[2 + NP + p], nm4, acc[r][2 + NP + p]);
    }
  }
#pragma unroll
  for (int r = R1; r < NRT; ++r)
#pragma unroll
    for (int p = 0; p < NP; ++p)
#pragma unroll
      for (int e = 0; e < 4; ++e) acc[r][2 + p][e] = __builtin_amdgcn_exp2f(acc[r][2 + p][e]);
#pragma unroll
  for (int r = R1; r < NRT; ++r) {
    const f4 sd4 = {sd[r], sd[r], sd[r], sd[r]};
#pragma unroll
    for (int p = 0; p < NP; ++p) acc[r][2 + p] = __builtin_elementwise_fma(acc[r][2 + p], sd4, sd4);
  }
#pragma unroll
  for (int r = R1; r < NRT; ++r)
#pragma unroll
    for (int p = 0; p < NP; ++p)
#pragma unroll
      for (int e = 0; e < 4; ++e) acc[r][2 + p][e] = __builtin_amdgcn_rcpf(acc[r][2 + p][e]);
#pragma unroll
  for (int r = R1; r < NRT; ++r) {
    const int rt = RT0 + r - 1;
#pragma unroll
    for (int p = 0; p < NP; ++p) {
      f4 d = acc[r][2 + p] * acc[r][2 + NP + p];
      u2 o; o[0] = pk2(d[0], d[1]); o[1] = pk2(d[2], d[3]);
      ST8O(hp[p] + (rt >> 2) * (68 * 4 * 512), o, (rt & 3) * 1024);
    }
  }
}

DEVI float xor1(float v) { return __builtin_bit_cast(float, __builtin_amdgcn_mov_dpp(__builtin_bit_cast(int, v), 0xB1, 0xf, 0xf, true)); }

__global__ __launch_bounds__(512, 2) void k1_kernel(const bf16_t* __restrict__ x, const bf16_t* __restrict__ wqkv,
                                                    const bf16_t* __restrict__ wg, const bf16_t* __restrict__ wu,
                                                    const bf16_t* __restrict__ wo, const bf16_t* __restrict__ wd,
                                                    bf16_t* __restrict__ A2, bf16_t* __restrict__ W2f) {
  __shared__ __attribute__((aligned(16))) char lds[DUMMY_OFF + 1024];
  const int tid = threadIdx.x, lane = tid & 63;
  const int w = __builtin_amdgcn_readfirstlane(tid >> 6);
  const int bid = blockIdx.x, xcd = bid & 7, idx = bid >> 3;
  const int rb = 8 * (xcd & 1) + (idx & 7), cg = 4 * (xcd >> 1) + (idx >> 3);
  const int r0 = rb * 128;
  K1Ctx c;
  {
    const int gw = bid * 8 + w;
    const char* pb = (const char*)g_ones; unsigned per = 0; int id = 0;
    if (gw < 400) { pb = (const char*)wqkv; per = 400; id = gw; }
    else if (gw < 720) { pb = (const char*)wg; per = 320; id = gw - 400; }
    else if (gw < 1040) { pb = (const char*)wu; per = 320; id = gw - 720; }
    else if (gw < 1466) { pb = (const char*)x; per = 426; id = gw - 1040; }
    else if (gw < 1599) { pb = (const char*)wo; per = 133; id = gw - 1466; }
    else if (gw < 1919) { pb = (const char*)wd; per = 320; id = gw - 1599; }
    c.pf = per ? pb + id * 1024 + lane * 16 : pb;
    c.pfs = per * 1024;
  }
#pragma unroll
  for (int i = 0; i < NPF0; ++i) k1_pf(c, lds);
  __builtin_amdgcn_sched_barrier(0);
  {
#pragma unroll
    for (int i = 0; i < 5; ++i) {
      const char* src = (const char*)g_ones; unsigned str = 0, kind = 2, dst = DUMMY_OFF;
      const int pb = w + 8 * i;
      const int pa = i == 2 ? 12 + w : w + 8 * (i - 3);
      if (i < 3 && pb < 20) {
        const unsigned ap = pb * 1024 + lane * 16;
        const unsigned k = ap / 640;
        const unsigned al = ap ^ ((((k >> 1) & 1) | (((k >> 3) & 1) << 1)) << 5);
        const unsigned ch = (al - k * 640) >> 4;
        kind = 0; dst = BOFF + pb * 1024;
        if (ch < 15) {
          src = (const char*)(wqkv + k * 1920 + ((ch * 13) >> 6) * 600 + 40 * cg + 8 * ch); str = 32u * 1920 * 2;
        } else if (ch > 15) {
          src = (const char*)((ch < 28 ? wg + 8 * (ch - 16) : wu + 8 * (ch - 28)) + k * 1536 + 96 * cg); str = 32u * 1536 * 2;
        }
      } else if (i >= 3 || (i == 2 && w < 6)) {
        const int lr = 8 * pa + (lane >> 3);
        int grow = r0 - 16 + lr;
        grow = grow < 0 ? 0 : grow;
        kind = 1; dst = pa * 1024;
        if (lr != 0) { src = (const char*)(x + grow * 640 + (((lane & 7) ^ (lr & 7)) * 8)); str = i == 2 ? 64u : 128u; }
      }
      c.lo[i] = (unsigned)(uint64_t)src; c.hi[i] = (unsigned)((uint64_t)src >> 32); c.str[i] = str;
      c.pdst[i] = dst; c.kind[i] = kind;
    }
  }
#pragma unroll
  for (int s = 0; s < 3; ++s) {
#pragma unroll
    for (int i = 0; i < ((s & 1) ? 3 : 5); ++i) k1_piece(c, lds, i, s * BSLOT, (s >> 1) * ASLOT, true, (s & 1) != 0);
    if (NPF0 + s < 6) k1_pf(c, lds);
    if (s < 2) __builtin_amdgcn_s_barrier();
  }
  {
    const int lst = w < 4 ? w : ((w + 2) & 3);
    if (w < 2) k1_main<0, 2>(c, lds, lane, lst, r0, cg, A2);
    else if (w < 4) k1_main<0, 1>(c, lds, lane, lst, r0, cg, A2);
    else if (w < 6) k1_main<1, 1>(c, lds, lane, lst, r0, cg, A2);
    else k1_main<1, 2>(c, lds, lane, lst, r0, cg, A2);
  }
  const int gw = bid * 8 + w;
  unsigned short wv[2][8];
#pragma unroll
  for (int t = 0; t < 2; ++t) {
    const int fi = gw + 2048 * t;
    if (t == 0 || fi < 2720) {
      const int ks = fi / 40, ct = fi - ks * 40;
      const bf16_t* p = (ks < 20 ? wo + ks * 32 * 640 : wd + (ks - 20) * 32 * 640) + (8 * (lane >> 4)) * 640 + 16 * ct + (lane & 15);
#pragma unroll
      for (int e = 0; e < 8; ++e) wv[t][e] = p[e * 640];
    }
  }
  LBAR();
  {
    const int half = tid & 1, pi = tid >> 1, hh = pi >> 7, row = pi & 127, pos = r0 + row;
    const char* pr = lds + (16 + row) * IMG_STRIDE + 40 * hh;
    const char* pw = pr - 8 * half * IMG_STRIDE;
    unsigned qv[10];
#pragma unroll
    for (int i = 0; i < 5; ++i) { u2 t = *(const u2*)(pr + 8 * i); qv[2 * i] = t[0]; qv[2 * i + 1] = t[1]; }
    float lg[4];
#pragma unroll
    for (int ww = 0; ww < 4; ++ww) {
      const char* pk = pw - 2 * ww * IMG_STRIDE + 80;
      float a = 0.f;
#pragma unroll
      for (int i = 0; i < 5; ++i) {
        u2 t = *(const u2*)(pk + 8 * i);
        a = dot2(qv[2 * i], t[0], a); a = dot2(qv[2 * i + 1], t[1], a);
      }
      a = bfr(bfr(a) * 0.22360679774997896f);
      lg[ww] = (pos - 2 * (4 * half + ww) >= 0) ? a : -1e9f;
    }
    float mx = fmaxf(fmaxf(lg[0], lg[1]), fmaxf(lg[2], lg[3]));
    mx = fmaxf(mx, xor1(mx));
    float sum = 0.f;
#pragma unroll
    for (int ww = 0; ww < 4; ++ww) { lg[ww] = __expf(lg[ww] - mx); sum += lg[ww]; }
    sum += xor1(sum);
    const float inv = __builtin_amdgcn_rcpf(sum);
    float o[20];
#pragma unroll
    for (int i = 0; i < 20; ++i) o[i] = 0.f;
#pragma unroll
    for (int wp = 0; wp < 2; ++wp) {
      const unsigned P = pk2(lg[2 * wp] * inv, lg[2 * wp + 1] * inv);
      const char* pv0 = pw - 4 * wp * IMG_STRIDE + 160;
      const char* pv1 = pv0 - 2 * IMG_STRIDE;
#pragma unroll
      for (int i = 0; i < 5; ++i) {
        u2 a = *(const u2*)(pv0 + 8 * i), b = *(const u2*)(pv1 + 8 * i);
#pragma unroll
        for (int e = 0; e < 2; ++e) {
          unsigned lo = __builtin_amdgcn_perm(b[e], a[e], 0x05040100u);
          unsigned hi = __builtin_amdgcn_perm(b[e], a[e], 0x07060302u);
          o[4 * i + 2 * e] = dot2(lo, P, o[4 * i + 2 * e]);
          o[4 * i + 2 * e + 1] = dot2(hi, P, o[4 * i + 2 * e + 1]);
        }
      }
    }
#pragma unroll
    for (int i = 0; i < 20; ++i) o[i] += xor1(o[i]);
    const int kc0 = 40 * cg + 20 * hh;
#pragma unroll
    for (int i = 0; i < 3; ++i) {
      const int pc = half ? 2 + i : i;
      u2 t;
      t[0] = half ? pk2(o[4 * (2 + i)], o[4 * (2 + i) + 1]) : pk2(o[4 * i], o[4 * i + 1]);
      t[1] = half ? pk2(o[4 * (2 + i) + 2], o[4 * (2 + i) + 3]) : pk2(o[4 * i + 2], o[4 * i + 3]);
      ST8(A2 + a2_off(pos, kc0 + 4 * pc), t);
    }
  }
#pragma unroll
  for (int t = 0; t < 2; ++t) {
    const int fi = gw + 2048 * t;
    if (t == 0 || fi < 2720) {
      u4 o;
#pragma unroll
      for (int e = 0; e < 4; ++e) o[e] = (unsigned)wv[t][2 * e] | ((unsigned)wv[t][2 * e + 1] << 16);
      ST16(W2f + ((size_t)fi * 64 + lane) * 8, o);
    }
  }
}


__global__ __launch_bounds__(256, 2) void k2_kernel(const bf16_t* __restrict__ A2, const bf16_t* __restrict__ W2f,
                                                    const bf16_t* __restrict__ x, bf16_t* __restrict__ out) {
  __shared__ __attribute__((aligned(16))) char lds[4 * 15 * 1024];
  const int tid = threadIdx.x, lane = tid & 63;
  const int w = __builtin_amdgcn_readfirstlane(tid >> 6);
  const int bid = blockIdx.x, xcd = bid & 7, idx = bid >> 3;
  const int rb64 = 8 * (xcd & 3) + (idx & 7), cb = 4 * (xcd >> 2) + (idx >> 3);
  f4 acc[4][5];
#pragma unroll
  for (int r = 0; r < 4; ++r)
#pragma unroll
    for (int q = 0; q < 5; ++q) acc[r][q] = f4{0.f, 0.f, 0.f, 0.f};
  const char* ap[4];
#pragma unroll
  for (int r = 0; r < 4; ++r) ap[r] = (const char*)(A2 + ((size_t)((rb64 * 68 + w) * 4 + ((r + w) & 3)) * 64 + lane) * 8);
  const char* bp = (const char*)(W2f + ((size_t)(w * 40 + 5 * cb) * 64 + lane) * 8);
  u4 fa[2][4], fb[2][5];
  auto issue = [&](int b) {
#pragma unroll
    for (int r = 0; r < 4; ++r) {
      asm volatile("global_load_dwordx4 %0, %1, off" : "=v"(fa[b][r]) : "v"(ap[r]));
      ap[r] += 4 * 4096;
    }
#pragma unroll
    for (int q = 0; q < 4; ++q)
      asm volatile("global_load_dwordx4 %0, %1, off offset:%2" : "=v"(fb[b][q]) : "v"(bp), "n"(q * 1024 - 2048));
    asm volatile("global_load_dwordx4 %0, %1, off offset:2048" : "=v"(fb[b][4]) : "v"(bp));
    bp += 4 * 40 * 1024;
  };
#define K2_WAIT(b, n) asm volatile("s_waitcnt vmcnt(" #n ")" : "+v"(fa[b][0]), "+v"(fa[b][1]), "+v"(fa[b][2]), "+v"(fa[b][3]), \
                 "+v"(fb[b][0]), "+v"(fb[b][1]), "+v"(fb[b][2]), "+v"(fb[b][3]), "+v"(fb[b][4]) :: "memory")
  auto mm = [&](int b) {
#pragma unroll
    for (int r = 3; r >= 0; --r)
#pragma unroll
      for (int ci = 0; ci < 5; ++ci) acc[r][ci] = MFMA(asbf(fb[b][ci]), asbf(fa[b][r]), acc[r][ci]);
  };
  const int grow = rb64 * 64 + 16 * w + (lane & 15), gcol = 80 * cb + 4 * (lane >> 4);
  u2 xv[5];
  bp += 2048;
  issue(0);
#pragma unroll 1
  for (int it = 0; it < 8; ++it) {
    issue(1);
    K2_WAIT(0, 9);
    mm(0);
    issue(0);
    K2_WAIT(1, 9);
    mm(1);
  }
  {
    const bf16_t* xp = x + grow * 640 + gcol;
#pragma unroll
    for (int ci = 0; ci < 5; ++ci)
      asm volatile("global_load_dwordx2 %0, %1, off offset:%2" : "=v"(xv[ci]) : "v"(xp), "n"(ci * 32));
  }
  K2_WAIT(0, 5);
  mm(0);
#pragma unroll
  for (int r = 3; r >= 1; --r) {
    char* d = lds + ((((r + w) & 3) * 3 + r - 1) * 5) * 1024 + lane * 16;
#pragma unroll
    for (int q = 0; q < 5; ++q) *(f4*)(d + q * 1024) = acc[r][q];
  }
  LBAR();
  asm volatile("s_waitcnt vmcnt(0)" : "+v"(xv[0]), "+v"(xv[1]), "+v"(xv[2]), "+v"(xv[3]), "+v"(xv[4]) :: "memory");
#pragma unroll
  for (int q = 0; q < 5; ++q) {
    f4 s = acc[0][q];
#pragma unroll
    for (int t = 0; t < 3; ++t) s += *(const f4*)(lds + ((w * 3 + t) * 5 + q) * 1024 + lane * 16);
    u2 o;
    o[0] = pk2(s[0] + lo16(xv[q][0]), s[1] + hi16(xv[q][0]));
    o[1] = pk2(s[2] + lo16(xv[q][1]), s[3] + hi16(xv[q][1]));
    *(u2*)(out + grow * 640 + gcol + 16 * q) = o;
  }
}

extern "C" void kernel_launch(void* const* d_in, const int* in_sizes, int n_in, void* d_out, int out_size,
                              void* d_ws, size_t ws_size, hipStream_t stream) {
  const bf16_t* x = (const bf16_t*)d_in[0];
  const bf16_t* wqkv = (const bf16_t*)d_in[1];
  const bf16_t* wo = (const bf16_t*)d_in[2];
  const bf16_t* wg = (const bf16_t*)d_in[4];
  const bf16_t* wu = (const bf16_t*)d_in[5];
  const bf16_t* wd = (const bf16_t*)d_in[6];
  bf16_t* A2 = (bf16_t*)d_ws;
  bf16_t* W2f = A2 + 6 * 1024 * 1024;
  k1_kernel<<<256, 512, 0, stream>>>(x, wqkv, wg, wu, wo, wd, A2, W2f);
  k2_kernel<<<256, 256, 0, stream>>>(A2, W2f, x, (bf16_t*)d_out);
}
```

```cpp
#include <hip/hip_runtime.h>
#include <hip/hip_bf16.h>
#include <stdint.h>

typedef __bf16 bf8 __attribute__((ext_vector_type(8)));
typedef __bf16 bf2 __attribute__((ext_vector_type(2)));
typedef float f4 __attribute__((ext_vector_type(4)));
typedef unsigned u4 __attribute__((ext_vector_type(4)));
typedef unsigned u2 __attribute__((ext_vector_type(2)));
typedef unsigned short bf16_t;

#define LAS __attribute__((address_space(3)))
#define GAS __attribute__((address_space(1)))
#define DEVI __device__ __forceinline__

__device__ const __attribute__((aligned(16))) unsigned g_ones[4] = {0x3F803F80u, 0x3F803F80u, 0x3F803F80u, 0x3F803F80u};

DEVI float bfr(float v) { return (float)(__bf16)v; }
DEVI unsigned pk2(float a, float b) { bf2 c; c[0] = (__bf16)a; c[1] = (__bf16)b; return __builtin_bit_cast(unsigned, c); }
DEVI float lo16(unsigned v) { return __builtin_bit_cast(float, v << 16); }
DEVI float hi16(unsigned v) { return __builtin_bit_cast(float, v & 0xffff0000u); }
DEVI float dot2(unsigned a, unsigned b, float c) {
  return __builtin_amdgcn_fdot2_f32_bf16(__builtin_bit_cast(bf2, a), __builtin_bit_cast(bf2, b), c, false);
}
DEVI void glds16(const void* g, unsigned ldsoff, char* lds) {
  __builtin_amdgcn_global_load_lds((const GAS void*)g, (LAS void*)(lds + ldsoff), 16, 0, 0);
}
#define RD128(d, addr, off) asm volatile("ds_read_b128 %0, %1 offset:%2" : "=v"(d) : "v"(addr), "n"(off))
#define RDTR(d, addr, off) asm volatile("ds_read_b64_tr_b16 %0, %1 offset:%2" : "=v"(d) : "v"(addr), "n"(off))
DEVI bf8 cat2(u2 a, u2 b) { u4 r; r[0] = a[0]; r[1] = a[1]; r[2] = b[0]; r[3] = b[1]; return __builtin_bit_cast(bf8, r); }
DEVI bf8 asbf(u4 a) { return __builtin_bit_cast(bf8, a); }
#define MFMA(a, b, c) __builtin_amdgcn_mfma_f32_16x16x32_bf16(a, b, c, 0, 0, 0)
#define WAITV(n) asm volatile("s_waitcnt vmcnt(" #n ")" ::: "memory")

#define ST8(p, v) asm volatile("global_store_dwordx2 %0, %1, off sc0 sc1" :: "v"(p), "v"(v) : "memory")
#define ST8O(p, v, o) asm volatile("global_store_dwordx2 %0, %1, off offset:%2 sc0 sc1" :: "v"(p), "v"(v), "n"(o) : "memory")
#define ST16(p, v) asm volatile("global_store_dwordx4 %0, %1, off sc0 sc1\n\ts_nop 1" :: "v"(p), "v"(v) : "memory")
constexpr int GM = 4;
constexpr int NPF0 = 3;
constexpr int ASLOT = 18432;
constexpr int BSLOT = 20480;
constexpr int BOFF = 3 * ASLOT;
constexpr int DUMMY_OFF = BOFF + 5 * BSLOT;
constexpr int IMG_STRIDE = 264;
constexpr int STAT_OFF = 144 * IMG_STRIDE;

DEVI unsigned a2_off(int grow, int kcol) {
  return (((((unsigned)grow >> 6) * 68 + ((unsigned)kcol >> 5)) * 4 + (((unsigned)grow >> 4) & 3)) * 64 +
          (((((unsigned)kcol >> 3) & 3) << 4) | ((unsigned)grow & 15))) * 8 + ((unsigned)kcol & 7);
}

struct K1Ctx {
  unsigned lo[5], hi[5];
  unsigned str[5];
  unsigned pdst[5];
  unsigned kind[5];
  const char* pf;
  unsigned pfs;
};
DEVI void k1_pf(K1Ctx& c, char* lds, uint64_t cbase, unsigned clen, unsigned& coff) {
  const char* p = clen ? (const char*)(cbase + (coff + 16 <= clen ? coff : 0u)) : c.pf;
  glds16(p, DUMMY_OFF, lds);
  c.pf += c.pfs;
  coff += 8192;
}

DEVI void k1_piece(K1Ctx& c, char* lds, int i, unsigned sb, unsigned sa, bool live, bool oddg) {
  const bool lv = (i == 2 && oddg) ? (live && c.kind[2] == 0) : live;
  const char* p = lv ? (const char*)(((uint64_t)c.hi[i] << 32) | c.lo[i]) : (const char*)g_ones;
  unsigned d;
  if (i < 2) d = sb + c.pdst[i];
  else if (i > 2) d = sa + c.pdst[i];
  else if (oddg) d = c.kind[2] == 0 ? sb + c.pdst[2] : (unsigned)DUMMY_OFF;
  else d = (c.kind[2] == 0 ? sb : c.kind[2] == 1 ? sa : 0u) + c.pdst[2];
  glds16(p, d, lds);
  asm("v_add_co_u32 %0, vcc, %0, %2\n\tv_addc_co_u32 %1, vcc, 0, %1, vcc" : "+v"(c.lo[i]), "+v"(c.hi[i]) : "v"(c.str[i]) : "vcc");
}
#define LBAR() do { asm volatile("s_waitcnt lgkmcnt(0)" ::: "memory"); __builtin_amdgcn_s_barrier(); asm volatile("" ::: "memory"); } while (0)

template <int MH, int NP>
DEVI void k1_main(K1Ctx& c, char* lds, int lane, int lst, int r0, int cg, bf16_t* __restrict__ A2) {
  constexpr int NRT = MH == 0 ? 5 : 4;
  constexpr int RT0 = MH == 0 ? 0 : 5;
  constexpr int NC = 2 + 2 * NP;
  constexpr int NG = NP == 2 ? 0 : (MH == 0 ? 3 : 2);
  constexpr int NA = NRT + NG;
  f4 acc[NRT][NC];
  f4 gacc[NG + 1];
#pragma unroll
  for (int r = 0; r < NRT; ++r)
#pragma unroll
    for (int q = 0; q < NC; ++q) acc[r][q] = f4{0.f, 0.f, 0.f, 0.f};
#pragma unroll
  for (int r = 0; r < NG + 1; ++r) gacc[r] = f4{0.f, 0.f, 0.f, 0.f};
  const unsigned lbase = (unsigned)(uintptr_t)(LAS char*)lds;
  const int ar = lane & 15, ac = lane >> 4;
  const unsigned abase0 = lbase + ar * 128 + ((ac ^ (ar & 7)) * 16);
  const unsigned abase1 = lbase + ar * 128 + (((4 + ac) ^ (ar & 7)) * 16);
  const int gt0 = MH == 0 ? 3 * (lst - 2) : 6 + (lst - 2);
  const int g = lane >> 4, q4 = (lane & 15) >> 2, p4 = lane & 3;
  const int p0 = NP == 2 ? 2 * lst : lst + 2;
  const unsigned hx = (((q4 >> 1) & 1) | ((g & 1) << 1)) << 5;
  auto baddr = [&](int ct) { return lbase + BOFF + ((((8 * g + q4) * 640 + 32 * ct) ^ hx) + 8 * p4); };
  unsigned bb[NC];
  bb[0] = baddr(2 * lst); bb[1] = baddr(2 * lst + 1);
#pragma unroll
  for (int p = 0; p < NP; ++p) { bb[2 + p] = baddr(8 + p0 + p); bb[2 + NP + p] = baddr(14 + p0 + p); }
  u4 fa[2][NA];
  u2 fb[2][NC][2];
  unsigned a0, g0, bc[NC];
  auto bases = [&](int odd, unsigned sb, unsigned sa) {
    a0 = (odd ? abase1 : abase0) + sa; g0 = a0 + gt0 * 2048;
#pragma unroll
    for (int ci = 0; ci < NC; ++ci) bc[ci] = bb[ci] + sb;
  };
  auto memop = [&](int nb, int k, int npc, unsigned sb, unsigned sa, bool live, bool liveA) {
    if (k < 2 * NC) {
      RDTR(fb[nb][k >> 1][k & 1], bc[k >> 1], (k & 1) * 2560);
    } else if (k < 2 * NC + NA) {
      const int r = k - 2 * NC;
      if (r < NRT) RD128(fa[nb][r], a0, (RT0 + r) * 2048);
      else RD128(fa[nb][r], g0, (r - NRT) * 2048);
    } else if (k < 2 * NC + NA + npc) {
      const int i = k - 2 * NC - NA;
      k1_piece(c, lds, i, sb, sa, live, npc == 3);
    }
  };
  auto wait_frags = [&](int buf) {
    asm volatile("s_waitcnt lgkmcnt(0)"
                 : "+v"(fa[buf][0]), "+v"(fa[buf][1]), "+v"(fa[buf][2]), "+v"(fa[buf][3]),
                   "+v"(fb[buf][0][0]), "+v"(fb[buf][0][1]), "+v"(fb[buf][1][0]), "+v"(fb[buf][1][1]), "+v"(fb[buf][2][0]),
                   "+v"(fb[buf][2][1]), "+v"(fb[buf][3][0]), "+v"(fb[buf][3][1])
                 :: "memory");
#pragma unroll
    for (int r = 4; r < NA; ++r) asm volatile("" : "+v"(fa[buf][r]) :: "memory");
#pragma unroll
    for (int q = 4; q < NC; ++q) asm volatile("" : "+v"(fb[buf][q][0]), "+v"(fb[buf][q][1]) :: "memory");
  };
  WAITV(8);
  __builtin_amdgcn_s_barrier();
  bases(0, 0, 0);
#pragma unroll
  for (int k = 0; k < 2 * NC + NA; ++k) memop(0, k, 0, 0, 0, true, true);
  unsigned sw = 0;
  unsigned sa = 0;
  auto step = [&](int b, bool live, unsigned san) {
    const unsigned sn = (sw == 4 * BSLOT) ? 0u : sw + BSLOT;
    const unsigned sap = (sa == 0) ? 2 * ASLOT : sa - ASLOT;
    const unsigned sp = sw >= 2 * BSLOT ? sw - 2 * BSLOT : sw + 3 * BSLOT;
    bases(b ^ 1, sn, b ? san : sa);
    if (b == 0) WAITV(5); else WAITV(3);
    wait_frags(b);
    __builtin_amdgcn_s_barrier();
    const int npc = b ? 5 : 3;
    auto mo = [&](int k) {
      if ((k % GM) == GM - 1 && (k / GM) < npc) memop(b ^ 1, 2 * NC + NA + (k / GM), npc, sp, sap, live, live);
      else { const int kr = k - ((k / GM) < npc ? (k / GM) : npc); if (kr < 2 * NC + NA) memop(b ^ 1, kr, npc, sp, sap, live, live); }
    };
    int k = 0;
#pragma unroll
    for (int ci = 0; ci < NC; ++ci)
#pragma unroll
      for (int r = 0; r < NRT; ++r) {
        acc[r][ci] = MFMA(cat2(fb[b][ci][0], fb[b][ci][1]), asbf(fa[b][r]), acc[r][ci]);
        mo(k); ++k;
        __builtin_amdgcn_sched_barrier(0);
      }
#pragma unroll
    for (int r = 0; r < NG; ++r) {
      gacc[r] = MFMA(asbf(fa[b][NRT + r]), asbf(fa[b][NRT + r]), gacc[r]);
      mo(k); ++k;
      __builtin_amdgcn_sched_barrier(0);
    }
#pragma unroll
    for (int kk = NC * NRT + NG; kk < 2 * NC + NA + 8; ++kk) mo(kk);
    sw = sn;
  };
#pragma unroll 1
  for (int s = 0; s < 20; s += 2) {
    const unsigned san = (sa == 2 * ASLOT) ? 0u : sa + ASLOT;
    step(0, s < 17, san);
    step(1, s + 1 < 17, san);
    sa = san;
  }
  asm volatile("s_waitcnt vmcnt(0) lgkmcnt(0)" ::: "memory");
  __builtin_amdgcn_s_barrier();
  float* st_rs = (float*)(lds + STAT_OFF);
  float* st_sq = st_rs + 144;
  float* st_cs = st_sq + 144;
  if constexpr (NG > 0) {
    const int rr = lane & 3;
    if (((lane & 15) >> 2) == (lane >> 4)) {
#pragma unroll
      for (int r = 0; r < NG; ++r)
        st_sq[16 * (gt0 + r) + (lane & 15)] = rr == 0 ? gacc[r][0] : rr == 1 ? gacc[r][1] : rr == 2 ? gacc[r][2] : gacc[r][3];
    }
    if (lst == 3 && (lane >> 4) == 2) {
#pragma unroll
      for (int r = 0; r < NRT; ++r) st_rs[16 * (RT0 + r) + (lane & 15)] = acc[r][1][0];
    }
  }
  int ctile[NC];
  ctile[0] = 2 * lst; ctile[1] = 2 * lst + 1;
#pragma unroll
  for (int p = 0; p < NP; ++p) { ctile[2 + p] = 8 + p0 + p; ctile[2 + NP + p] = 14 + p0 + p; }
  if constexpr (MH == 0) {
    if ((lane & 15) == 0) {
#pragma unroll
      for (int ci = 0; ci < NC; ++ci) *(f4*)(st_cs + 16 * ctile[ci] + 4 * (lane >> 4)) = acc[0][ci];
    }
  }
  LBAR();
  f4 cs[NC];
  bf16_t* hp[NP];
#pragma unroll
  for (int p = 0; p < NP; ++p) hp[p] = A2 + a2_off(r0 + (lane & 15), 640 + 96 * cg + 16 * (p0 + p) + 4 * (lane >> 4));
#pragma unroll
  for (int ci = 0; ci < NC; ++ci) cs[ci] = *(const f4*)(st_cs + 16 * ctile[ci] + 4 * (lane >> 4));
  float nm[NRT], sd[NRT], rinv[NRT];
#pragma unroll
  for (int r = 0; r < NRT; ++r) {
    const int row = 16 * (RT0 + r) + (lane & 15);
    const float mean = st_rs[row] * (1.0f / 640.0f);
    const float var = fmaxf(st_sq[row] * (1.0f / 640.0f) - mean * mean, 0.f);
    nm[r] = -bfr(mean);
    sd[r] = bfr(__builtin_amdgcn_sqrtf(bfr(bfr(var) + 1e-5f)));
    rinv[r] = __builtin_amdgcn_rcpf(sd[r]);
  }
#pragma unroll
  for (int r = 0; r < NRT; ++r) {
    const int row = 16 * (RT0 + r) + (lane & 15);
    const f4 nm4 = {nm[r], nm[r], nm[r], nm[r]};
#pragma unroll
    for (int ci = 0; ci < 2; ++ci) {
      f4 y = __builtin_elementwise_fma(cs[ci], nm4, acc[r][ci]) * rinv[r];
      u2 o; o[0] = pk2(y[0], y[1]); o[1] = pk2(y[2], y[3]);
      *(u2*)(lds + row * IMG_STRIDE + (16 * ctile[ci] + 4 * (lane >> 4)) * 2) = o;
    }
  }
  constexpr int R1 = MH == 0 ? 1 : 0;
#pragma unroll
  for (int r = R1; r < NRT; ++r) {
    const f4 nm4 = {nm[r], nm[r], nm[r], nm[r]};
    const float ga = -1.4426950408889634f * rinv[r];
#pragma unroll
    for (int p = 0; p < NP; ++p) {
      acc[r][2 + p] = __builtin_elementwise_fma(cs[2 + p], nm4, acc[r][2 + p]) * ga;
      acc[r][2 + NP + p] = __builtin_elementwise_fma(cs[2 + NP + p], nm4, acc[r][2 + NP + p]);
    }
  }
#pragma unroll
  for (int r = R1; r < NRT; ++r)
#pragma unroll
    for (int p = 0; p < NP; ++p)
#pragma unroll
      for (int e = 0; e < 4; ++e) acc[r][2 + p][e] = __builtin_amdgcn_exp2f(acc[r][2 + p][e]);
#pragma unroll
  for (int r = R1; r < NRT; ++r) {
    const f4 sd4 = {sd[r], sd[r], sd[r], sd[r]};
#pragma unroll
    for (int p = 0; p < NP; ++p) acc[r][2 + p] = __builtin_elementwise_fma(acc[r][2 + p], sd4, sd4);
  }
#pragma unroll
  for (int r = R1; r < NRT; ++r)
#pragma unroll
    for (int p = 0; p < NP; ++p)
#pragma unroll
      for (int e = 0; e < 4; ++e) acc[r][2 + p][e] = __builtin_amdgcn_rcpf(acc[r][2 + p][e]);
#pragma unroll
  for (int r = R1; r < NRT; ++r) {
    const int rt = RT0 + r - 1;
#pragma unroll
    for (int p = 0; p < NP; ++p) {
      f4 d = acc[r][2 + p] * acc[r][2 + NP + p];
      u2 o; o[0] = pk2(d[0], d[1]); o[1] = pk2(d[2], d[3]);
      ST8O(hp[p] + (rt >> 2) * (68 * 4 * 512), o, (rt & 3) * 1024);
    }
  }
}

DEVI float xor1(float v) { return __builtin_bit_cast(float, __builtin_amdgcn_mov_dpp(__builtin_bit_cast(int, v), 0xB1, 0xf, 0xf, true)); }

__global__ __launch_bounds__(512, 2) void k1_kernel(const bf16_t* __restrict__ x, const bf16_t* __restrict__ wqkv,
                                                    const bf16_t* __restrict__ wg, const bf16_t* __restrict__ wu,
                                                    const bf16_t* __restrict__ wo, const bf16_t* __restrict__ wd,
                                                    bf16_t* __restrict__ A2, bf16_t* __restrict__ W2f) {
  __shared__ __attribute__((aligned(16))) char lds[DUMMY_OFF + 1024];
  const int tid = threadIdx.x, lane = tid & 63;
  const int w = __builtin_amdgcn_readfirstlane(tid >> 6);
  const int bid = blockIdx.x, xcd = bid & 7, idx = bid >> 3;
  const int rb = 8 * (xcd & 1) + (idx & 7), cg = 4 * (xcd >> 1) + (idx >> 3);
  const int r0 = rb * 128;
  K1Ctx c;
  uint64_t cpf_base = 0; unsigned cpf_len = 0, cpf_off = 0;
  {
    const int gw = bid * 8 + w;
    const char* pb = (const char*)g_ones; unsigned per = 0; int id = 0;
    if (gw < 400) { pb = (const char*)wqkv; per = 400; id = gw; }
    else if (gw < 720) { pb = (const char*)wg; per = 320; id = gw - 400; }
    else if (gw < 1040) { pb = (const char*)wu; per = 320; id = gw - 720; }
    else if (gw < 1466) { pb = (const char*)x; per = 426; id = gw - 1040; }
    else if (gw < 1599) { pb = (const char*)wo; per = 133; id = gw - 1466; }
    else if (gw < 1919) { pb = (const char*)wd; per = 320; id = gw - 1599; }
    c.pf = per ? pb + id * 1024 + lane * 16 : pb;
    c.pfs = per * 1024;
    if (gw >= 1920) {
      unsigned plo, phi, qlo, qhi;
      asm volatile("s_getpc_b64 s[40:41]\n\ts_mov_b32 %0, s40\n\ts_mov_b32 %1, s41\n\t"
                   "s_getpc_b64 s[42:43]\n\ts_add_u32 s42, s42, _Z9k2_kernelPKtS0_S0_Pt@rel32@lo+4\n\t"
                   "s_addc_u32 s43, s43, _Z9k2_kernelPKtS0_S0_Pt@rel32@hi+12\n\ts_mov_b32 %2, s42\n\ts_mov_b32 %3, s43"
                   : "=s"(plo), "=s"(phi), "=s"(qlo), "=s"(qhi) :: "s40", "s41", "s42", "s43", "scc");
      const uint64_t pc = (((uint64_t)phi << 32) | plo) & ~(uint64_t)15, k2a = ((uint64_t)qhi << 32) | qlo;
      const uint64_t len = k2a + 2560 - pc;
      const unsigned lw = ((bid - 240) >> 3) * 8 + w;
      if (k2a > pc && len < 6 * 8192) {
        const uint64_t off0 = (uint64_t)(lw & 7) * 1024 + lane * 16;
        cpf_base = pc; cpf_len = (unsigned)len; cpf_off = (unsigned)off0;
      }
    }
  }
#pragma unroll
  for (int i = 0; i < NPF0; ++i) k1_pf(c, lds, cpf_base, cpf_len, cpf_off);
  __builtin_amdgcn_sched_barrier(0);
  {
#pragma unroll
    for (int i = 0; i < 5; ++i) {
      const char* src = (const char*)g_ones; unsigned str = 0, kind = 2, dst = DUMMY_OFF;
      const int pb = w + 8 * i;
      const int pa = i == 2 ? 12 + w : w + 8 * (i - 3);
      if (i < 3 && pb < 20) {
        const unsigned ap = pb * 1024 + lane * 16;
        const unsigned k = ap / 640;
        const unsigned al = ap ^ ((((k >> 1) & 1) | (((k >> 3) & 1) << 1)) << 5);
        const unsigned ch = (al - k * 640) >> 4;
        kind = 0; dst = BOFF + pb * 1024;
        if (ch < 15) {
          src = (const char*)(wqkv + k * 1920 + ((ch * 13) >> 6) * 600 + 40 * cg + 8 * ch); str = 32u * 1920 * 2;
        } else if (ch > 15) {
          src = (const char*)((ch < 28 ? wg + 8 * (ch - 16) : wu + 8 * (ch - 28)) + k * 1536 + 96 * cg); str = 32u * 1536 * 2;
        }
      } else if (i >= 3 || (i == 2 && w < 6)) {
        const int lr = 8 * pa + (lane >> 3);
        int grow = r0 - 16 + lr;
        grow = grow < 0 ? 0 : grow;
        kind = 1; dst = pa * 1024;
        if (lr != 0) { src = (const char*)(x + grow * 640 + (((lane & 7) ^ (lr & 7)) * 8)); str = i == 2 ? 64u : 128u; }
      }
      c.lo[i] = (unsigned)(uint64_t)src; c.hi[i] = (unsigned)((uint64_t)src >> 32); c.str[i] = str;
      c.pdst[i] = dst; c.kind[i] = kind;
    }
  }
#pragma unroll
  for (int s = 0; s < 3; ++s) {
#pragma unroll
    for (int i = 0; i < ((s & 1) ? 3 : 5); ++i) k1_piece(c, lds, i, s * BSLOT, (s >> 1) * ASLOT, true, (s & 1) != 0);
    if (NPF0 + s < 6) k1_pf(c, lds, cpf_base, cpf_len, cpf_off);
    if (s < 2) __builtin_amdgcn_s_barrier();
  }
  {
    const int lst = w < 4 ? w : ((w + 2) & 3);
    if (w < 2) k1_main<0, 2>(c, lds, lane, lst, r0, cg, A2);
    else if (w < 4) k1_main<0, 1>(c, lds, lane, lst, r0, cg, A2);
    else if (w < 6) k1_main<1, 1>(c, lds, lane, lst, r0, cg, A2);
    else k1_main<1, 2>(c, lds, lane, lst, r0, cg, A2);
  }
  const int gw = bid * 8 + w;
  unsigned short wv[2][8];
#pragma unroll
  for (int t = 0; t < 2; ++t) {
    const int fi = gw + 2048 * t;
    if (t == 0 || fi < 2720) {
      const int ks = fi / 40, ct = fi - ks * 40;
      const bf16_t* p = (ks < 20 ? wo + ks * 32 * 640 : wd + (ks - 20) * 32 * 640) + (8 * (lane >> 4)) * 640 + 16 * ct + (lane & 15);
#pragma unroll
      for (int e = 0; e < 8; ++e) wv[t][e] = p[e * 640];
    }
  }
  LBAR();
  {
    const int half = tid & 1, pi = tid >> 1, hh = pi >> 7, row = pi & 127, pos = r0 + row;
    const char* pr = lds + (16 + row) * IMG_STRIDE + 40 * hh;
    const char* pw = pr - 8 * half * IMG_STRIDE;
    unsigned qv[10];
#pragma unroll
    for (int i = 0; i < 5; ++i) { u2 t = *(const u2*)(pr + 8 * i); qv[2 * i] = t[0]; qv[2 * i + 1] = t[1]; }
    float lg[4];
#pragma unroll
    for (int ww = 0; ww < 4; ++ww) {
      const char* pk = pw - 2 * ww * IMG_STRIDE + 80;
      float a = 0.f;
#pragma unroll
      for (int i = 0; i < 5; ++i) {
        u2 t = *(const u2*)(pk + 8 * i);
        a = dot2(qv[2 * i], t[0], a); a = dot2(qv[2 * i + 1], t[1], a);
      }
      a = bfr(bfr(a) * 0.22360679774997896f);
      lg[ww] = (pos - 2 * (4 * half + ww) >= 0) ? a : -1e9f;
    }
    float mx = fmaxf(fmaxf(lg[0], lg[1]), fmaxf(lg[2], lg[3]));
    mx = fmaxf(mx, xor1(mx));
    float sum = 0.f;
#pragma unroll
    for (int ww = 0; ww < 4; ++ww) { lg[ww] = __expf(lg[ww] - mx); sum += lg[ww]; }
    sum += xor1(sum);
    const float inv = __builtin_amdgcn_rcpf(sum);
    float o[20];
#pragma unroll
    for (int i = 0; i < 20; ++i) o[i] = 0.f;
#pragma unroll
    for (int wp = 0; wp < 2; ++wp) {
      const unsigned P = pk2(lg[2 * wp] * inv, lg[2 * wp + 1] * inv);
      const char* pv0 = pw - 4 * wp * IMG_STRIDE + 160;
      const char* pv1 = pv0 - 2 * IMG_STRIDE;
#pragma unroll
      for (int i = 0; i < 5; ++i) {
        u2 a = *(const u2*)(pv0 + 8 * i), b = *(const u2*)(pv1 + 8 * i);
#pragma unroll
        for (int e = 0; e < 2; ++e) {
          unsigned lo = __builtin_amdgcn_perm(b[e], a[e], 0x05040100u);
          unsigned hi = __builtin_amdgcn_perm(b[e], a[e], 0x07060302u);
          o[4 * i + 2 * e] = dot2(lo, P, o[4 * i + 2 * e]);
          o[4 * i + 2 * e + 1] = dot2(hi, P, o[4 * i + 2 * e + 1]);
        }
      }
    }
#pragma unroll
    for (int i = 0; i < 20; ++i) o[i] += xor1(o[i]);
    const int kc0 = 40 * cg + 20 * hh;
#pragma unroll
    for (int i = 0; i < 3; ++i) {
      const int pc = half ? 2 + i : i;
      u2 t;
      t[0] = half ? pk2(o[4 * (2 + i)], o[4 * (2 + i) + 1]) : pk2(o[4 * i], o[4 * i + 1]);
      t[1] = half ? pk2(o[4 * (2 + i) + 2], o[4 * (2 + i) + 3]) : pk2(o[4 * i + 2], o[4 * i + 3]);
      ST8(A2 + a2_off(pos, kc0 + 4 * pc), t);
    }
  }
#pragma unroll
  for (int t = 0; t < 2; ++t) {
    const int fi = gw + 2048 * t;
    if (t == 0 || fi < 2720) {
      u4 o;
#pragma unroll
      for (int e = 0; e < 4; ++e) o[e] = (unsigned)wv[t][2 * e] | ((unsigned)wv[t][2 * e + 1] << 16);
      ST16(W2f + ((size_t)fi * 64 + lane) * 8, o);
    }
  }
}


__global__ __launch_bounds__(256, 2) void k2_kernel(const bf16_t* __restrict__ A2, const bf16_t* __restrict__ W2f,
                                                    const bf16_t* __restrict__ x, bf16_t* __restrict__ out) {
  __shared__ __attribute__((aligned(16))) char lds[4 * 15 * 1024];
  const int tid = threadIdx.x, lane = tid & 63;
  const int w = __builtin_amdgcn_readfirstlane(tid >> 6);
  const int bid = blockIdx.x, xcd = bid & 7, idx = bid >> 3;
  const int rb64 = 8 * (xcd & 3) + (idx & 7), cb = 4 * (xcd >> 2) + (idx >> 3);
  f4 acc[4][5];
#pragma unroll
  for (int r = 0; r < 4; ++r)
#pragma unroll
    for (int q = 0; q < 5; ++q) acc[r][q] = f4{0.f, 0.f, 0.f, 0.f};
  const char* ap[4];
#pragma unroll
  for (int r = 0; r < 4; ++r) ap[r] = (const char*)(A2 + ((size_t)((rb64 * 68 + w) * 4 + ((r + w) & 3)) * 64 + lane) * 8);
  const char* bp = (const char*)(W2f + ((size_t)(w * 40 + 5 * cb) * 64 + lane) * 8);
  u4 fa[2][4], fb[2][5];
  auto issue = [&](int b) {
#pragma unroll
    for (int r = 0; r < 4; ++r) {
      asm volatile("global_load_dwordx4 %0, %1, off" : "=v"(fa[b][r]) : "v"(ap[r]));
      ap[r] += 4 * 4096;
    }
#pragma unroll
    for (int q = 0; q < 4; ++q)
      asm volatile("global_load_dwordx4 %0, %1, off offset:%2" : "=v"(fb[b][q]) : "v"(bp), "n"(q * 1024 - 2048));
    asm volatile("global_load_dwordx4 %0, %1, off offset:2048" : "=v"(fb[b][4]) : "v"(bp));
    bp += 4 * 40 * 1024;
  };
#define K2_WAIT(b, n) asm volatile("s_waitcnt vmcnt(" #n ")" : "+v"(fa[b][0]), "+v"(fa[b][1]), "+v"(fa[b][2]), "+v"(fa[b][3]), \
                 "+v"(fb[b][0]), "+v"(fb[b][1]), "+v"(fb[b][2]), "+v"(fb[b][3]), "+v"(fb[b][4]) :: "memory")
  auto mm = [&](int b) {
#pragma unroll
    for (int r = 3; r >= 0; --r)
#pragma unroll
      for (int ci = 0; ci < 5; ++ci) acc[r][ci] = MFMA(asbf(fb[b][ci]), asbf(fa[b][r]), acc[r][ci]);
  };
  const int grow = rb64 * 64 + 16 * w + (lane & 15), gcol = 80 * cb + 4 * (lane >> 4);
  u2 xv[5];
  bp += 2048;
  issue(0);
#pragma unroll 1
  for (int it = 0; it < 8; ++it) {
    issue(1);
    K2_WAIT(0, 9);
    mm(0);
    issue(0);
    K2_WAIT(1, 9);
    mm(1);
  }
  {
    const bf16_t* xp = x + grow * 640 + gcol;
#pragma unroll
    for (int ci = 0; ci < 5; ++ci)
      asm volatile("global_load_dwordx2 %0, %1, off offset:%2" : "=v"(xv[ci]) : "v"(xp), "n"(ci * 32));
  }
  K2_WAIT(0, 5);
  mm(0);
#pragma unroll
  for (int r = 3; r >= 1; --r) {
    char* d = lds + ((((r + w) & 3) * 3 + r - 1) * 5) * 1024 + lane * 16;
#pragma unroll
    for (int q = 0; q < 5; ++q) *(f4*)(d + q * 1024) = acc[r][q];
  }
  LBAR();
  asm volatile("s_waitcnt vmcnt(0)" : "+v"(xv[0]), "+v"(xv[1]), "+v"(xv[2]), "+v"(xv[3]), "+v"(xv[4]) :: "memory");
#pragma unroll
  for (int q = 0; q < 5; ++q) {
    f4 s = acc[0][q];
#pragma unroll
    for (int t = 0; t < 3; ++t) s += *(const f4*)(lds + ((w * 3 + t) * 5 + q) * 1024 + lane * 16);
    u2 o;
    o[0] = pk2(s[0] + lo16(xv[q][0]), s[1] + hi16(xv[q][0]));
    o[1] = pk2(s[2] + lo16(xv[q][1]), s[3] + hi16(xv[q][1]));
    *(u2*)(out + grow * 640 + gcol + 16 * q) = o;
  }
}

extern "C" void kernel_launch(void* const* d_in, const int* in_sizes, int n_in, void* d_out, int out_size,
                              void* d_ws, size_t ws_size, hipStream_t stream) {
  const bf16_t* x = (const bf16_t*)d_in[0];
  const bf16_t* wqkv = (const bf16_t*)d_in[1];
  const bf16_t* wo = (const bf16_t*)d_in[2];
  const bf16_t* wg = (const bf16_t*)d_in[4];
  const bf16_t* wu = (const bf16_t*)d_in[5];
  const bf16_t* wd = (const bf16_t*)d_in[6];
  bf16_t* A2 = (bf16_t*)d_ws;
  bf16_t* W2f = A2 + 6 * 1024 * 1024;
  k1_kernel<<<256, 512, 0, stream>>>(x, wqkv, wg, wu, wo, wd, A2, W2f);
  k2_kernel<<<256, 256, 0, stream>>>(A2, W2f, x, (bf16_t*)d_out);
}
```

```cpp
#include <hip/hip_runtime.h>
#include <hip/hip_bf16.h>
#include <stdint.h>

typedef __bf16 bf8 __attribute__((ext_vector_type(8)));
typedef __bf16 bf2 __attribute__((ext_vector_type(2)));
typedef float f4 __attribute__((ext_vector_type(4)));
typedef unsigned u4 __attribute__((ext_vector_type(4)));
typedef unsigned u2 __attribute__((ext_vector_type(2)));
typedef unsigned short bf16_t;

#define LAS __attribute__((address_space(3)))
#define GAS __attribute__((address_space(1)))
#define DEVI __device__ __forceinline__

__device__ const __attribute__((aligned(16))) unsigned g_ones[4] = {0x3F803F80u, 0x3F803F80u, 0x3F803F80u, 0x3F803F80u};

DEVI float bfr(float v) { return (float)(__bf16)v; }
DEVI unsigned pk2(float a, float b) { bf2 c; c[0] = (__bf16)a; c[1] = (__bf16)b; return __builtin_bit_cast(unsigned, c); }
DEVI float lo16(unsigned v) { return __builtin_bit_cast(float, v << 16); }
DEVI float hi16(unsigned v) { return __builtin_bit_cast(float, v & 0xffff0000u); }
DEVI float dot2(unsigned a, unsigned b, float c) {
  return __builtin_amdgcn_fdot2_f32_bf16(__builtin_bit_cast(bf2, a), __builtin_bit_cast(bf2, b), c, false);
}
DEVI void glds16(const void* g, unsigned ldsoff, char* lds) {
  __builtin_amdgcn_global_load_lds((const GAS void*)g, (LAS void*)(lds + ldsoff), 16, 0, 0);
}
#define RD128(d, addr, off) asm volatile("ds_read_b128 %0, %1 offset:%2" : "=v"(d) : "v"(addr), "n"(off))
#define RDTR(d, addr, off) asm volatile("ds_read_b64_tr_b16 %0, %1 offset:%2" : "=v"(d) : "v"(addr), "n"(off))
DEVI bf8 cat2(u2 a, u2 b) { u4 r; r[0] = a[0]; r[1] = a[1]; r[2] = b[0]; r[3] = b[1]; return __builtin_bit_cast(bf8, r); }
DEVI bf8 asbf(u4 a) { return __builtin_bit_cast(bf8, a); }
#define MFMA(a, b, c) __builtin_amdgcn_mfma_f32_16x16x32_bf16(a, b, c, 0, 0, 0)
#define WAITV(n) asm volatile("s_waitcnt vmcnt(" #n ")" ::: "memory")

#define ST8(p, v) asm volatile("global_store_dwordx2 %0, %1, off sc0 sc1" :: "v"(p), "v"(v) : "memory")
#define ST8O(p, v, o) asm volatile("global_store_dwordx2 %0, %1, off offset:%2 sc0 sc1" :: "v"(p), "v"(v), "n"(o) : "memory")
#define ST16(p, v) asm volatile("global_store_dwordx4 %0, %1, off sc0 sc1\n\ts_nop 1" :: "v"(p), "v"(v) : "memory")
constexpr int GM = 4;
constexpr int NPF0 = 3;
constexpr int ASLOT = 18432;
constexpr int BSLOT = 20480;
constexpr int BOFF = 3 * ASLOT;
constexpr int DUMMY_OFF = BOFF + 5 * BSLOT;
constexpr int IMG_STRIDE = 264;
constexpr int STAT_OFF = 144 * IMG_STRIDE;

DEVI unsigned a2_off(int grow, int kcol) {
  return (((((unsigned)grow >> 6) * 68 + ((unsigned)kcol >> 5)) * 4 + (((unsigned)grow >> 4) & 3)) * 64 +
          (((((unsigned)kcol >> 3) & 3) << 4) | ((unsigned)grow & 15))) * 8 + ((unsigned)kcol & 7);
}

struct K1Ctx {
  unsigned lo[5], hi[5];
  unsigned str[5];
  unsigned pdst[5];
  unsigned kind[5];
  const char* pf;
  unsigned pfs;
};
DEVI void k1_pf(K1Ctx& c, char* lds) {
  glds16(c.pf, DUMMY_OFF, lds);
  c.pf += c.pfs;
}

DEVI void k1_piece(K1Ctx& c, char* lds, int i, unsigned sb, unsigned sa, bool live, bool oddg) {
  const bool lv = (i == 2 && oddg) ? (live && c.kind[2] == 0) : live;
  const char* p = lv ? (const char*)(((uint64_t)c.hi[i] << 32) | c.lo[i]) : (const char*)g_ones;
  unsigned d;
  if (i < 2) d = sb + c.pdst[i];
  else if (i > 2) d = sa + c.pdst[i];
  else if (oddg) d = c.kind[2] == 0 ? sb + c.pdst[2] : (unsigned)DUMMY_OFF;
  else d = (c.kind[2] == 0 ? sb : c.kind[2] == 1 ? sa : 0u) + c.pdst[2];
  glds16(p, d, lds);
  asm("v_add_co_u32 %0, vcc, %0, %2\n\tv_addc_co_u32 %1, vcc, 0, %1, vcc" : "+v"(c.lo[i]), "+v"(c.hi[i]) : "v"(c.str[i]) : "vcc");
}
#define LBAR() do { asm volatile("s_waitcnt lgkmcnt(0)" ::: "memory"); __builtin_amdgcn_s_barrier(); asm volatile("" ::: "memory"); } while (0)

template <int MH, int NP>
DEVI void k1_main(K1Ctx& c, char* lds, int lane, int lst, int r0, int cg, bf16_t* __restrict__ A2) {
  constexpr int NRT = MH == 0 ? 5 : 4;
  constexpr int RT0 = MH == 0 ? 0 : 5;
  constexpr int NC = 2 + 2 * NP;
  constexpr int NG = NP == 2 ? 0 : (MH == 0 ? 3 : 2);
  constexpr int NA = NRT + NG;
  f4 acc[NRT][NC];
  f4 gacc[NG + 1];
#pragma unroll
  for (int r = 0; r < NRT; ++r)
#pragma unroll
    for (int q = 0; q < NC; ++q) acc[r][q] = f4{0.f, 0.f, 0.f, 0.f};
#pragma unroll
  for (int r = 0; r < NG + 1; ++r) gacc[r] = f4{0.f, 0.f, 0.f, 0.f};
  const unsigned lbase = (unsigned)(uintptr_t)(LAS char*)lds;
  const int ar = lane & 15, ac = lane >> 4;
  const unsigned abase0 = lbase + ar * 128 + ((ac ^ (ar & 7)) * 16);
  const unsigned abase1 = lbase + ar * 128 + (((4 + ac) ^ (ar & 7)) * 16);
  const int gt0 = MH == 0 ? 3 * (lst - 2) : 6 + (lst - 2);
  const int g = lane >> 4, q4 = (lane & 15) >> 2, p4 = lane & 3;
  const int p0 = NP == 2 ? 2 * lst : lst + 2;
  const unsigned hx = (((q4 >> 1) & 1) | ((g & 1) << 1)) << 5;
  auto baddr = [&](int ct) { return lbase + BOFF + ((((8 * g + q4) * 640 + 32 * ct) ^ hx) + 8 * p4); };
  unsigned bb[NC];
  bb[0] = baddr(2 * lst); bb[1] = baddr(2 * lst + 1);
#pragma unroll
  for (int p = 0; p < NP; ++p) { bb[2 + p] = baddr(8 + p0 + p); bb[2 + NP + p] = baddr(14 + p0 + p); }
  u4 fa[2][NA];
  u2 fb[2][NC][2];
  unsigned a0, g0, bc[NC];
  auto bases = [&](int odd, unsigned sb, unsigned sa) {
    a0 = (odd ? abase1 : abase0) + sa; g0 = a0 + gt0 * 2048;
#pragma unroll
    for (int ci = 0; ci < NC; ++ci) bc[ci] = bb[ci] + sb;
  };
  auto memop = [&](int nb, int k, int npc, unsigned sb, unsigned sa, bool live, bool liveA) {
    if (k < 2 * NC) {
      RDTR(fb[nb][k >> 1][k & 1], bc[k >> 1], (k & 1) * 2560);
    } else if (k < 2 * NC + NA) {
      const int r = k - 2 * NC;
      if (r < NRT) RD128(fa[nb][r], a0, (RT0 + r) * 2048);
      else RD128(fa[nb][r], g0, (r - NRT) * 2048);
    } else if (k < 2 * NC + NA + npc) {
      const int i = k - 2 * NC - NA;
      k1_piece(c, lds, i, sb, sa, live, npc == 3);
    }
  };
  auto wait_frags = [&](int buf) {
    asm volatile("s_waitcnt lgkmcnt(0)"
                 : "+v"(fa[buf][0]), "+v"(fa[buf][1]), "+v"(fa[buf][2]), "+v"(fa[buf][3]),
                   "+v"(fb[buf][0][0]), "+v"(fb[buf][0][1]), "+v"(fb[buf][1][0]), "+v"(fb[buf][1][1]), "+v"(fb[buf][2][0]),
                   "+v"(fb[buf][2][1]), "+v"(fb[buf][3][0]), "+v"(fb[buf][3][1])
                 :: "memory");
#pragma unroll
    for (int r = 4; r < NA; ++r) asm volatile("" : "+v"(fa[buf][r]) :: "memory");
#pragma unroll
    for (int q = 4; q < NC; ++q) asm volatile("" : "+v"(fb[buf][q][0]), "+v"(fb[buf][q][1]) :: "memory");
  };
  WAITV(8);
  __builtin_amdgcn_s_barrier();
  bases(0, 0, 0);
#pragma unroll
  for (int k = 0; k < 2 * NC + NA; ++k) memop(0, k, 0, 0, 0, true, true);
  unsigned sw = 0;
  unsigned sa = 0;
  auto step = [&](int b, bool live, unsigned san) {
    const unsigned sn = (sw == 4 * BSLOT) ? 0u : sw + BSLOT;
    const unsigned sap = (sa == 0) ? 2 * ASLOT : sa - ASLOT;
    const unsigned sp = sw >= 2 * BSLOT ? sw - 2 * BSLOT : sw + 3 * BSLOT;
    bases(b ^ 1, sn, b ? san : sa);
    if (b == 0) WAITV(5); else WAITV(3);
    wait_frags(b);
    __builtin_amdgcn_s_barrier();
    const int npc = b ? 5 : 3;
    auto mo = [&](int k) {
      if ((k % GM) == GM - 1 && (k / GM) < npc) memop(b ^ 1, 2 * NC + NA + (k / GM), npc, sp, sap, live, live);
      else { const int kr = k - ((k / GM) < npc ? (k / GM) : npc); if (kr < 2 * NC + NA) memop(b ^ 1, kr, npc, sp, sap, live, live); }
    };
    int k = 0;
#pragma unroll
    for (int ci = 0; ci < NC; ++ci)
#pragma unroll
      for (int r = 0; r < NRT; ++r) {
        acc[r][ci] = MFMA(cat2(fb[b][ci][0], fb[b][ci][1]), asbf(fa[b][r]), acc[r][ci]);
        mo(k); ++k;
        __builtin_amdgcn_sched_barrier(0);
      }
#pragma unroll
    for (int r = 0; r < NG; ++r) {
      gacc[r] = MFMA(asbf(fa[b][NRT + r]), asbf(fa[b][NRT + r]), gacc[r]);
      mo(k); ++k;
      __builtin_amdgcn_sched_barrier(0);
    }
#pragma unroll
    for (int kk = NC * NRT + NG; kk < 2 * NC + NA + 8; ++kk) mo(kk);
    sw = sn;
  };
#pragma unroll 1
  for (int s = 0; s < 20; s += 2) {
    const unsigned san = (sa == 2 * ASLOT) ? 0u : sa + ASLOT;
    step(0, s < 17, san);
    step(1, s + 1 < 17, san);
    sa = san;
  }
  asm volatile("s_waitcnt vmcnt(0) lgkmcnt(0)" ::: "memory");
  __builtin_amdgcn_s_barrier();
  float* st_rs = (float*)(lds + STAT_OFF);
  float* st_sq = st_rs + 144;
  float* st_cs = st_sq + 144;
  if constexpr (NG > 0) {
    const int rr = lane & 3;
    if (((lane & 15) >> 2) == (lane >> 4)) {
#pragma unroll
      for (int r = 0; r < NG; ++r)
        st_sq[16 * (gt0 + r) + (lane & 15)] = rr == 0 ? gacc[r][0] : rr == 1 ? gacc[r][1] : rr == 2 ? gacc[r][2] : gacc[r][3];
    }
    if (lst == 3 && (lane >> 4) == 2) {
#pragma unroll
      for (int r = 0; r < NRT; ++r) st_rs[16 * (RT0 + r) + (lane & 15)] = acc[r][1][0];
    }
  }
  int ctile[NC];
  ctile[0] = 2 * lst; ctile[1] = 2 * lst + 1;
#pragma unroll
  for (int p = 0; p < NP; ++p) { ctile[2 + p] = 8 + p0 + p; ctile[2 + NP + p] = 14 + p0 + p; }
  if constexpr (MH == 0) {
    if ((lane & 15) == 0) {
#pragma unroll
      for (int ci = 0; ci < NC; ++ci) *(f4*)(st_cs + 16 * ctile[ci] + 4 * (lane >> 4)) = acc[0][ci];
    }
  }
  LBAR();
  f4 cs[NC];
  bf16_t* hp[NP];
#pragma unroll
  for (int p = 0; p < NP; ++p) hp[p] = A2 + a2_off(r0 + (lane & 15), 640 + 96 * cg + 16 * (p0 + p) + 4 * (lane >> 4));
#pragma unroll
  for (int ci = 0; ci < NC; ++ci) cs[ci] = *(const f4*)(st_cs + 16 * ctile[ci] + 4 * (lane >> 4));
  float nm[NRT], sd[NRT], rinv[NRT];
#pragma unroll
  for (int r = 0; r < NRT; ++r) {
    const int row = 16 * (RT0 + r) + (lane & 15);
    const float mean = st_rs[row] * (1.0f / 640.0f);
    const float var = fmaxf(st_sq[row] * (1.0f / 640.0f) - mean * mean, 0.f);
    nm[r] = -bfr(mean);
    sd[r] = bfr(__builtin_amdgcn_sqrtf(bfr(bfr(var) + 1e-5f)));
    rinv[r] = __builtin_amdgcn_rcpf(sd[r]);
  }
#pragma unroll
  for (int r = 0; r < NRT; ++r) {
    const int row = 16 * (RT0 + r) + (lane & 15);
    const f4 nm4 = {nm[r], nm[r], nm[r], nm[r]};
#pragma unroll
    for (int ci = 0; ci < 2; ++ci) {
      f4 y = __builtin_elementwise_fma(cs[ci], nm4, acc[r][ci]) * rinv[r];
      u2 o; o[0] = pk2(y[0], y[1]); o[1] = pk2(y[2], y[3]);
      *(u2*)(lds + row * IMG_STRIDE + (16 * ctile[ci] + 4 * (lane >> 4)) * 2) = o;
    }
  }
  constexpr int R1 = MH == 0 ? 1 : 0;
#pragma unroll
  for (int r = R1; r < NRT; ++r) {
    const f4 nm4 = {nm[r], nm[r], nm[r], nm[r]};
    const float ga = -1.4426950408889634f * rinv[r];
#pragma unroll
    for (int p = 0; p < NP; ++p) {
      acc[r][2 + p] = __builtin_elementwise_fma(cs[2 + p], nm4, acc[r][2 + p]) * ga;
      acc[r][2 + NP + p] = __builtin_elementwise_fma(cs[2 + NP + p], nm4, acc[r][2 + NP + p]);
    }
  }
#pragma unroll
  for (int r = R1; r < NRT; ++r)
#pragma unroll
    for (int p = 0; p < NP; ++p)
#pragma unroll
      for (int e = 0; e < 4; ++e) acc[r][2 + p][e] = __builtin_amdgcn_exp2f(acc[r][2 + p][e]);
#pragma unroll
  for (int r = R1; r < NRT; ++r) {
    const f4 sd4 = {sd[r], sd[r], sd[r], sd[r]};
#pragma unroll
    for (int p = 0; p < NP; ++p) acc[r][2 + p] = __builtin_elementwise_fma(acc[r][2 + p], sd4, sd4);
  }
#pragma unroll
  for (int r = R1; r < NRT; ++r)
#pragma unroll
    for (int p = 0; p < NP; ++p)
#pragma unroll
      for (int e = 0; e < 4; ++e) acc[r][2 + p][e] = __builtin_amdgcn_rcpf(acc[r][2 + p][e]);
#pragma unroll
  for (int r = R1; r < NRT; ++r) {
    const int rt = RT0 + r - 1;
#pragma unroll
    for (int p = 0; p < NP; ++p) {
      f4 d = acc[r][2 + p] * acc[r][2 + NP + p];
      u2 o; o[0] = pk2(d[0], d[1]); o[1] = pk2(d[2], d[3]);
      ST8O(hp[p] + (rt >> 2) * (68 * 4 * 512), o, (rt & 3) * 1024);
    }
  }
}

DEVI float xor1(float v) { return __builtin_bit_cast(float, __builtin_amdgcn_mov_dpp(__builtin_bit_cast(int, v), 0xB1, 0xf, 0xf, true)); }

__global__ __launch_bounds__(512, 2) void k1_kernel(const bf16_t* __restrict__ x, const bf16_t* __restrict__ wqkv,
                                                    const bf16_t* __restrict__ wg, const bf16_t* __restrict__ wu,
                                                    const bf16_t* __restrict__ wo, const bf16_t* __restrict__ wd,
                                                    bf16_t* __restrict__ A2, bf16_t* __restrict__ W2f) {
  __shared__ __attribute__((aligned(16))) char lds[DUMMY_OFF + 1024];
  const int tid = threadIdx.x, lane = tid & 63;
  const int w = __builtin_amdgcn_readfirstlane(tid >> 6);
  const int bid = blockIdx.x, xcd = bid & 7, idx = bid >> 3;
  const int rb = 8 * (xcd & 1) + (idx & 7), cg = 4 * (xcd >> 1) + (idx >> 3);
  const int r0 = rb * 128;
  K1Ctx c;
  {
    const int gw = bid * 8 + w;
    const char* pb = (const char*)g_ones; unsigned per = 0; int id = 0;
    if (gw < 400) { pb = (const char*)wqkv; per = 400; id = gw; }
    else if (gw < 720) { pb = (const char*)wg; per = 320; id = gw - 400; }
    else if (gw < 1040) { pb = (const char*)wu; per = 320; id = gw - 720; }
    else if (gw < 1466) { pb = (const char*)x; per = 426; id = gw - 1040; }
    else if (gw < 1599) { pb = (const char*)wo; per = 133; id = gw - 1466; }
    else if (gw < 1919) { pb = (const char*)wd; per = 320; id = gw - 1599; }
    c.pf = per ? pb + id * 1024 + lane * 16 : pb;
    c.pfs = per * 1024;
  }
#pragma unroll
  for (int i = 0; i < NPF0; ++i) k1_pf(c, lds);
  __builtin_amdgcn_sched_barrier(0);
  {
#pragma unroll
    for (int i = 0; i < 5; ++i) {
      const char* src = (const char*)g_ones; unsigned str = 0, kind = 2, dst = DUMMY_OFF;
      const int pb = w + 8 * i;
      const int pa = i == 2 ? 12 + w : w + 8 * (i - 3);
      if (i < 3 && pb < 20) {
        const unsigned ap = pb * 1024 + lane * 16;
        const unsigned k = ap / 640;
        const unsigned al = ap ^ ((((k >> 1) & 1) | (((k >> 3) & 1) << 1)) << 5);
        const unsigned ch = (al - k * 640) >> 4;
        kind = 0; dst = BOFF + pb * 1024;
        if (ch < 15) {
          src = (const char*)(wqkv + k * 1920 + ((ch * 13) >> 6) * 600 + 40 * cg + 8 * ch); str = 32u * 1920 * 2;
        } else if (ch > 15) {
          src = (const char*)((ch < 28 ? wg + 8 * (ch - 16) : wu + 8 * (ch - 28)) + k * 1536 + 96 * cg); str = 32u * 1536 * 2;
        }
      } else if (i >= 3 || (i == 2 && w < 6)) {
        const int lr = 8 * pa + (lane >> 3);
        int grow = r0 - 16 + lr;
        grow = grow < 0 ? 0 : grow;
        kind = 1; dst = pa * 1024;
        if (lr != 0) { src = (const char*)(x + grow * 640 + (((lane & 7) ^ (lr & 7)) * 8)); str = i == 2 ? 64u : 128u; }
      }
      c.lo[i] = (unsigned)(uint64_t)src; c.hi[i] = (unsigned)((uint64_t)src >> 32); c.str[i] = str;
      c.pdst[i] = dst; c.kind[i] = kind;
    }
  }
#pragma unroll
  for (int s = 0; s < 3; ++s) {
#pragma unroll
    for (int i = 0; i < ((s & 1) ? 3 : 5); ++i) k1_piece(c, lds, i, s * BSLOT, (s >> 1) * ASLOT, true, (s & 1) != 0);
    if (NPF0 + s < 6) k1_pf(c, lds);
    if (s < 2) __builtin_amdgcn_s_barrier();
  }
  {
    const int lst = w < 4 ? w : ((w + 2) & 3);
    if (w < 2) k1_main<0, 2>(c, lds, lane, lst, r0, cg, A2);
    else if (w < 4) k1_main<0, 1>(c, lds, lane, lst, r0, cg, A2);
    else if (w < 6) k1_main<1, 1>(c, lds, lane, lst, r0, cg, A2);
    else k1_main<1, 2>(c, lds, lane, lst, r0, cg, A2);
  }
  const int gw = bid * 8 + w;
  unsigned short wv[2][8];
#pragma unroll
  for (int t = 0; t < 2; ++t) {
    const int fi = gw + 2048 * t;
    if (t == 0 || fi < 2720) {
      const int ks = fi / 40, ct = fi - ks * 40;
      const bf16_t* p = (ks < 20 ? wo + ks * 32 * 640 : wd + (ks - 20) * 32 * 640) + (8 * (lane >> 4)) * 640 + 16 * ct + (lane & 15);
#pragma unroll
      for (int e = 0; e < 8; ++e) wv[t][e] = p[e * 640];
    }
  }
  LBAR();
  {
    const int half = tid & 1, pi = tid >> 1, hh = pi >> 7, row = pi & 127, pos = r0 + row;
    const char* pr = lds + (16 + row) * IMG_STRIDE + 40 * hh;
    const char* pw = pr - 8 * half * IMG_STRIDE;
    unsigned qv[10];
#pragma unroll
    for (int i = 0; i < 5; ++i) { u2 t = *(const u2*)(pr + 8 * i); qv[2 * i] = t[0]; qv[2 * i + 1] = t[1]; }
    float lg[4];
#pragma unroll
    for (int ww = 0; ww < 4; ++ww) {
      const char* pk = pw - 2 * ww * IMG_STRIDE + 80;
      float a = 0.f;
#pragma unroll
      for (int i = 0; i < 5; ++i) {
        u2 t = *(const u2*)(pk + 8 * i);
        a = dot2(qv[2 * i], t[0], a); a = dot2(qv[2 * i + 1], t[1], a);
      }
      a = bfr(bfr(a) * 0.22360679774997896f);
      lg[ww] = (pos - 2 * (4 * half + ww) >= 0) ? a : -1e9f;
    }
    float mx = fmaxf(fmaxf(lg[0], lg[1]), fmaxf(lg[2], lg[3]));
    mx = fmaxf(mx, xor1(mx));
    float sum = 0.f;
#pragma unroll
    for (int ww = 0; ww < 4; ++ww) { lg[ww] = __expf(lg[ww] - mx); sum += lg[ww]; }
    sum += xor1(sum);
    const float inv = __builtin_amdgcn_rcpf(sum);
    float o[20];
#pragma unroll
    for (int i = 0; i < 20; ++i) o[i] = 0.f;
#pragma unroll
    for (int wp = 0; wp < 2; ++wp) {
      const unsigned P = pk2(lg[2 * wp] * inv, lg[2 * wp + 1] * inv);
      const char* pv0 = pw - 4 * wp * IMG_STRIDE + 160;
      const char* pv1 = pv0 - 2 * IMG_STRIDE;
#pragma unroll
      for (int i = 0; i < 5; ++i) {
        u2 a = *(const u2*)(pv0 + 8 * i), b = *(const u2*)(pv1 + 8 * i);
#pragma unroll
        for (int e = 0; e < 2; ++e) {
          unsigned lo = __builtin_amdgcn_perm(b[e], a[e], 0x05040100u);
          unsigned hi = __builtin_amdgcn_perm(b[e], a[e], 0x07060302u);
          o[4 * i + 2 * e] = dot2(lo, P, o[4 * i + 2 * e]);
          o[4 * i + 2 * e + 1] = dot2(hi, P, o[4 * i + 2 * e + 1]);
        }
      }
    }
#pragma unroll
    for (int i = 0; i < 20; ++i) o[i] += xor1(o[i]);
    const int kc0 = 40 * cg + 20 * hh;
    unsigned pk[10];
#pragma unroll
    for (int i = 0; i < 10; ++i) pk[i] = pk2(o[2 * i], o[2 * i + 1]);
    const unsigned mh = 0u - (unsigned)hh, mf = 0u - (unsigned)half;
    u4 cv; u2 pv;
#pragma unroll
    for (int e = 0; e < 4; ++e) {
      const unsigned lo = pk[e] ^ ((pk[e] ^ pk[e + 2]) & mh), hi = pk[4 + e] ^ ((pk[4 + e] ^ pk[6 + e]) & mh);
      cv[e] = lo ^ ((lo ^ hi) & mf);
    }
    pv[0] = pk[8] ^ ((pk[8] ^ pk[0]) & mh);
    pv[1] = pk[9] ^ ((pk[9] ^ pk[1]) & mh);
    ST16(A2 + a2_off(pos, kc0 + 4 * hh + 8 * half), cv);
    if (half == 0) ST8(A2 + a2_off(pos, kc0 + 16 * (1 - hh)), pv);
  }
#pragma unroll
  for (int t = 0; t < 2; ++t) {
    const int fi = gw + 2048 * t;
    if (t == 0 || fi < 2720) {
      u4 o;
#pragma unroll
      for (int e = 0; e < 4; ++e) o[e] = (unsigned)wv[t][2 * e] | ((unsigned)wv[t][2 * e + 1] << 16);
      ST16(W2f + ((size_t)fi * 64 + lane) * 8, o);
    }
  }
}


__global__ __launch_bounds__(256, 2) void k2_kernel(const bf16_t* __restrict__ A2, const bf16_t* __restrict__ W2f,
                                                    const bf16_t* __restrict__ x, bf16_t* __restrict__ out) {
  __shared__ __attribute__((aligned(16))) char lds[4 * 15 * 1024];
  const int tid = threadIdx.x, lane = tid & 63;
  const int w = __builtin_amdgcn_readfirstlane(tid >> 6);
  const int bid = blockIdx.x, xcd = bid & 7, idx = bid >> 3;
  const int rb64 = 8 * (xcd & 3) + (idx & 7), cb = 4 * (xcd >> 2) + (idx >> 3);
  f4 acc[4][5];
#pragma unroll
  for (int r = 0; r < 4; ++r)
#pragma unroll
    for (int q = 0; q < 5; ++q) acc[r][q] = f4{0.f, 0.f, 0.f, 0.f};
  const char* ap[4];
#pragma unroll
  for (int r = 0; r < 4; ++r) ap[r] = (const char*)(A2 + ((size_t)((rb64 * 68 + w) * 4 + ((r + w) & 3)) * 64 + lane) * 8);
  const char* bp = (const char*)(W2f + ((size_t)(w * 40 + 5 * cb) * 64 + lane) * 8);
  u4 fa[2][4], fb[2][5];
  auto issue = [&](int b) {
#pragma unroll
    for (int r = 0; r < 4; ++r) {
      asm volatile("global_load_dwordx4 %0, %1, off" : "=v"(fa[b][r]) : "v"(ap[r]));
      ap[r] += 4 * 4096;
    }
#pragma unroll
    for (int q = 0; q < 4; ++q)
      asm volatile("global_load_dwordx4 %0, %1, off offset:%2" : "=v"(fb[b][q]) : "v"(bp), "n"(q * 1024 - 2048));
    asm volatile("global_load_dwordx4 %0, %1, off offset:2048" : "=v"(fb[b][4]) : "v"(bp));
    bp += 4 * 40 * 1024;
  };
#define K2_WAIT(b, n) asm volatile("s_waitcnt vmcnt(" #n ")" : "+v"(fa[b][0]), "+v"(fa[b][1]), "+v"(fa[b][2]), "+v"(fa[b][3]), \
                 "+v"(fb[b][0]), "+v"(fb[b][1]), "+v"(fb[b][2]), "+v"(fb[b][3]), "+v"(fb[b][4]) :: "memory")
  auto mm = [&](int b) {
#pragma unroll
    for (int r = 3; r >= 0; --r)
#pragma unroll
      for (int ci = 0; ci < 5; ++ci) acc[r][ci] = MFMA(asbf(fb[b][ci]), asbf(fa[b][r]), acc[r][ci]);
  };
  const int grow = rb64 * 64 + 16 * w + (lane & 15), gcol = 80 * cb + 4 * (lane >> 4);
  u2 xv[5];
  bp += 2048;
  issue(0);
#pragma unroll 1
  for (int it = 0; it < 8; ++it) {
    issue(1);
    K2_WAIT(0, 9);
    mm(0);
    issue(0);
    K2_WAIT(1, 9);
    mm(1);
  }
  {
    const bf16_t* xp = x + grow * 640 + gcol;
#pragma unroll
    for (int ci = 0; ci < 5; ++ci)
      asm volatile("global_load_dwordx2 %0, %1, off offset:%2" : "=v"(xv[ci]) : "v"(xp), "n"(ci * 32));
  }
  K2_WAIT(0, 5);
  mm(0);
#pragma unroll
  for (int r = 3; r >= 1; --r) {
    char* d = lds + ((((r + w) & 3) * 3 + r - 1) * 5) * 1024 + lane * 16;
#pragma unroll
    for (int q = 0; q < 5; ++q) *(f4*)(d + q * 1024) = acc[r][q];
  }
  LBAR();
  asm volatile("s_waitcnt vmcnt(0)" : "+v"(xv[0]), "+v"(xv[1]), "+v"(xv[2]), "+v"(xv[3]), "+v"(xv[4]) :: "memory");
#pragma unroll
  for (int q = 0; q < 5; ++q) {
    f4 s = acc[0][q];
#pragma unroll
    for (int t = 0; t < 3; ++t) s += *(const f4*)(lds + ((w * 3 + t) * 5 + q) * 1024 + lane * 16);
    u2 o;
    o[0] = pk2(s[0] + lo16(xv[q][0]), s[1] + hi16(xv[q][0]));
    o[1] = pk2(s[2] + lo16(xv[q][1]), s[3] + hi16(xv[q][1]));
    *(u2*)(out + grow * 640 + gcol + 16 * q) = o;
  }
}

extern "C" void kernel_launch(void* const* d_in, const int* in_sizes, int n_in, void* d_out, int out_size,
                              void* d_ws, size_t ws_size, hipStream_t stream) {
  const bf16_t* x = (const bf16_t*)d_in[0];
  const bf16_t* wqkv = (const bf16_t*)d_in[1];
  const bf16_t* wo = (const bf16_t*)d_in[2];
  const bf16_t* wg = (const bf16_t*)d_in[4];
  const bf16_t* wu = (const bf16_t*)d_in[5];
  const bf16_t* wd = (const bf16_t*)d_in[6];
  bf16_t* A2 = (bf16_t*)d_ws;
  bf16_t* W2f = A2 + 6 * 1024 * 1024;
  k1_kernel<<<256, 512, 0, stream>>>(x, wqkv, wg, wu, wo, wd, A2, W2f);
  k2_kernel<<<256, 256, 0, stream>>>(A2, W2f, x, (bf16_t*)d_out);
}
```

```cpp
#include <hip/hip_runtime.h>
#include <hip/hip_bf16.h>
#include <stdint.h>

typedef __bf16 bf8 __attribute__((ext_vector_type(8)));
typedef __bf16 bf2 __attribute__((ext_vector_type(2)));
typedef float f4 __attribute__((ext_vector_type(4)));
typedef unsigned u4 __attribute__((ext_vector_type(4)));
typedef unsigned u2 __attribute__((ext_vector_type(2)));
typedef unsigned short bf16_t;

#define LAS __attribute__((address_space(3)))
#define GAS __attribute__((address_space(1)))
#define DEVI __device__ __forceinline__

__device__ const __attribute__((aligned(16))) unsigned g_ones[4] = {0x3F803F80u, 0x3F803F80u, 0x3F803F80u, 0x3F803F80u};

DEVI float bfr(float v) { return (float)(__bf16)v; }
DEVI unsigned pk2(float a, float b) { bf2 c; c[0] = (__bf16)a; c[1] = (__bf16)b; return __builtin_bit_cast(unsigned, c); }
DEVI float lo16(unsigned v) { return __builtin_bit_cast(float, v << 16); }
DEVI float hi16(unsigned v) { return __builtin_bit_cast(float, v & 0xffff0000u); }
DEVI float dot2(unsigned a, unsigned b, float c) {
  return __builtin_amdgcn_fdot2_f32_bf16(__builtin_bit_cast(bf2, a), __builtin_bit_cast(bf2, b), c, false);
}
DEVI void glds16(const void* g, unsigned ldsoff, char* lds) {
  __builtin_amdgcn_global_load_lds((const GAS void*)g, (LAS void*)(lds + ldsoff), 16, 0, 0);
}
#define RD128(d, addr, off) asm volatile("ds_read_b128 %0, %1 offset:%2" : "=v"(d) : "v"(addr), "n"(off))
#define RDTR(d, addr, off) asm volatile("ds_read_b64_tr_b16 %0, %1 offset:%2" : "=v"(d) : "v"(addr), "n"(off))
DEVI bf8 cat2(u2 a, u2 b) { u4 r; r[0] = a[0]; r[1] = a[1]; r[2] = b[0]; r[3] = b[1]; return __builtin_bit_cast(bf8, r); }
DEVI bf8 asbf(u4 a) { return __builtin_bit_cast(bf8, a); }
#define MFMA(a, b, c) __builtin_amdgcn_mfma_f32_16x16x32_bf16(a, b, c, 0, 0, 0)
#define WAITV(n) asm volatile("s_waitcnt vmcnt(" #n ")" ::: "memory")

#define ST8(p, v) asm volatile("global_store_dwordx2 %0, %1, off sc0 sc1" :: "v"(p), "v"(v) : "memory")
#define ST8O(p, v, o) asm volatile("global_store_dwordx2 %0, %1, off offset:%2 sc0 sc1" :: "v"(p), "v"(v), "n"(o) : "memory")
#define ST16(p, v) asm volatile("global_store_dwordx4 %0, %1, off sc0 sc1\n\ts_nop 1" :: "v"(p), "v"(v) : "memory")
constexpr int GM = 4;
constexpr int NPF0 = 3;
constexpr int ASLOT = 18432;
constexpr int BSLOT = 20480;
constexpr int BOFF = 3 * ASLOT;
constexpr int DUMMY_OFF = BOFF + 5 * BSLOT;
constexpr int IMG_STRIDE = 264;
constexpr int STAT_OFF = 144 * IMG_STRIDE;

DEVI unsigned a2_off(int grow, int kcol) {
  return (((((unsigned)grow >> 6) * 68 + ((unsigned)kcol >> 5)) * 4 + (((unsigned)grow >> 4) & 3)) * 64 +
          (((((unsigned)kcol >> 3) & 3) << 4) | ((unsigned)grow & 15))) * 8 + ((unsigned)kcol & 7);
}

struct K1Ctx {
  unsigned lo[5], hi[5];
  unsigned str[5];
  unsigned pdst[5];
  unsigned kind[5];
  const char* pf;
  unsigned pfs;
};
DEVI void k1_pf(K1Ctx& c, char* lds) {
  glds16(c.pf, DUMMY_OFF, lds);
  c.pf += c.pfs;
}

DEVI void k1_piece(K1Ctx& c, char* lds, int i, unsigned sb, unsigned sa, bool live, bool oddg) {
  const bool lv = (i == 2 && oddg) ? (live && c.kind[2] == 0) : live;
  const char* p = lv ? (const char*)(((uint64_t)c.hi[i] << 32) | c.lo[i]) : (const char*)g_ones;
  unsigned d;
  if (i < 2) d = sb + c.pdst[i];
  else if (i > 2) d = sa + c.pdst[i];
  else if (oddg) d = c.kind[2] == 0 ? sb + c.pdst[2] : (unsigned)DUMMY_OFF;
  else d = (c.kind[2] == 0 ? sb : c.kind[2] == 1 ? sa : 0u) + c.pdst[2];
  glds16(p, d, lds);
  asm("v_add_co_u32 %0, vcc, %0, %2\n\tv_addc_co_u32 %1, vcc, 0, %1, vcc" : "+v"(c.lo[i]), "+v"(c.hi[i]) : "v"(c.str[i]) : "vcc");
}
#define LBAR() do { asm volatile("s_waitcnt lgkmcnt(0)" ::: "memory"); __builtin_amdgcn_s_barrier(); asm volatile("" ::: "memory"); } while (0)

template <int MH, int NP>
DEVI void k1_main(K1Ctx& c, char* lds, int lane, int lst, int r0, int cg, bf16_t* __restrict__ A2) {
  constexpr int NRT = MH == 0 ? 5 : 4;
  constexpr int RT0 = MH == 0 ? 0 : 5;
  constexpr int NC = 2 + 2 * NP;
  constexpr int NG = NP == 2 ? 0 : (MH == 0 ? 3 : 2);
  constexpr int NA = NRT + NG;
  f4 acc[NRT][NC];
  f4 gacc[NG + 1];
#pragma unroll
  for (int r = 0; r < NRT; ++r)
#pragma unroll
    for (int q = 0; q < NC; ++q) acc[r][q] = f4{0.f, 0.f, 0.f, 0.f};
#pragma unroll
  for (int r = 0; r < NG + 1; ++r) gacc[r] = f4{0.f, 0.f, 0.f, 0.f};
  const unsigned lbase = (unsigned)(uintptr_t)(LAS char*)lds;
  const int ar = lane & 15, ac = lane >> 4;
  const unsigned abase0 = lbase + ar * 128 + ((ac ^ (ar & 7)) * 16);
  const unsigned abase1 = lbase + ar * 128 + (((4 + ac) ^ (ar & 7)) * 16);
  const int gt0 = MH == 0 ? 3 * (lst - 2) : 6 + (lst - 2);
  const int g = lane >> 4, q4 = (lane & 15) >> 2, p4 = lane & 3;
  const int p0 = NP == 2 ? 2 * lst : lst + 2;
  const unsigned hx = (((q4 >> 1) & 1) | ((g & 1) << 1)) << 5;
  auto baddr = [&](int ct) { return lbase + BOFF + ((((8 * g + q4) * 640 + 32 * ct) ^ hx) + 8 * p4); };
  unsigned bb[NC];
  bb[0] = baddr(2 * lst); bb[1] = baddr(2 * lst + 1);
#pragma unroll
  for (int p = 0; p < NP; ++p) { bb[2 + p] = baddr(8 + p0 + p); bb[2 + NP + p] = baddr(14 + p0 + p); }
  u4 fa[2][NA];
  u2 fb[2][NC][2];
  unsigned a0, g0, bc[NC];
  auto bases = [&](int odd, unsigned sb, unsigned sa) {
    a0 = (odd ? abase1 : abase0) + sa; g0 = a0 + gt0 * 2048;
#pragma unroll
    for (int ci = 0; ci < NC; ++ci) bc[ci] = bb[ci] + sb;
  };
  auto memop = [&](int nb, int k, int npc, unsigned sb, unsigned sa, bool live, bool liveA) {
    if (k < 2 * NC) {
      RDTR(fb[nb][k >> 1][k & 1], bc[k >> 1], (k & 1) * 2560);
    } else if (k < 2 * NC + NA) {
      const int r = k - 2 * NC;
      if (r < NRT) RD128(fa[nb][r], a0, (RT0 + r) * 2048);
      else RD128(fa[nb][r], g0, (r - NRT) * 2048);
    } else if (k < 2 * NC + NA + npc) {
      const int i = k - 2 * NC - NA;
      k1_piece(c, lds, i, sb, sa, live, npc == 3);
    }
  };
  auto wait_frags = [&](int buf) {
    asm volatile("s_waitcnt lgkmcnt(0)"
                 : "+v"(fa[buf][0]), "+v"(fa[buf][1]), "+v"(fa[buf][2]), "+v"(fa[buf][3]),
                   "+v"(fb[buf][0][0]), "+v"(fb[buf][0][1]), "+v"(fb[buf][1][0]), "+v"(fb[buf][1][1]), "+v"(fb[buf][2][0]),
                   "+v"(fb[buf][2][1]), "+v"(fb[buf][3][0]), "+v"(fb[buf][3][1])
                 :: "memory");
#pragma unroll
    for (int r = 4; r < NA; ++r) asm volatile("" : "+v"(fa[buf][r]) :: "memory");
#pragma unroll
    for (int q = 4; q < NC; ++q) asm volatile("" : "+v"(fb[buf][q][0]), "+v"(fb[buf][q][1]) :: "memory");
  };
  WAITV(8);
  __builtin_amdgcn_s_barrier();
  bases(0, 0, 0);
#pragma unroll
  for (int k = 0; k < 2 * NC + NA; ++k) memop(0, k, 0, 0, 0, true, true);
  unsigned sw = 0;
  unsigned sa = 0;
  auto step = [&](int b, bool live, unsigned san) {
    const unsigned sn = (sw == 4 * BSLOT) ? 0u : sw + BSLOT;
    const unsigned sap = (sa == 0) ? 2 * ASLOT : sa - ASLOT;
    const unsigned sp = sw >= 2 * BSLOT ? sw - 2 * BSLOT : sw + 3 * BSLOT;
    bases(b ^ 1, sn, b ? san : sa);
    if (b == 0) WAITV(5); else WAITV(3);
    wait_frags(b);
    __builtin_amdgcn_s_barrier();
    const int npc = b ? 5 : 3;
    auto mo = [&](int k) {
      if ((k % GM) == GM - 1 && (k / GM) < npc) memop(b ^ 1, 2 * NC + NA + (k / GM), npc, sp, sap, live, live);
      else { const int kr = k - ((k / GM) < npc ? (k / GM) : npc); if (kr < 2 * NC + NA) memop(b ^ 1, kr, npc, sp, sap, live, live); }
    };
    int k = 0;
#pragma unroll
    for (int ci = 0; ci < NC; ++ci)
#pragma unroll
      for (int r = 0; r < NRT; ++r) {
        acc[r][ci] = MFMA(cat2(fb[b][ci][0], fb[b][ci][1]), asbf(fa[b][r]), acc[r][ci]);
        mo(k); ++k;
        __builtin_amdgcn_sched_barrier(0);
      }
#pragma unroll
    for (int r = 0; r < NG; ++r) {
      gacc[r] = MFMA(asbf(fa[b][NRT + r]), asbf(fa[b][NRT + r]), gacc[r]);
      mo(k); ++k;
      __builtin_amdgcn_sched_barrier(0);
    }
#pragma unroll
    for (int kk = NC * NRT + NG; kk < 2 * NC + NA + 8; ++kk) mo(kk);
    sw = sn;
  };
#pragma unroll 1
  for (int s = 0; s < 20; s += 2) {
    const unsigned san = (sa == 2 * ASLOT) ? 0u : sa + ASLOT;
    step(0, s < 17, san);
    step(1, s + 1 < 17, san);
    sa = san;
  }
  asm volatile("s_waitcnt vmcnt(0) lgkmcnt(0)" ::: "memory");
  __builtin_amdgcn_s_barrier();
  float* st_rs = (float*)(lds + STAT_OFF);
  float* st_sq = st_rs + 144;
  float* st_cs = st_sq + 144;
  if constexpr (NG > 0) {
    const int rr = lane & 3;
    if (((lane & 15) >> 2) == (lane >> 4)) {
#pragma unroll
      for (int r = 0; r < NG; ++r)
        st_sq[16 * (gt0 + r) + (lane & 15)] = rr == 0 ? gacc[r][0] : rr == 1 ? gacc[r][1] : rr == 2 ? gacc[r][2] : gacc[r][3];
    }
    if (lst == 3 && (lane >> 4) == 2) {
#pragma unroll
      for (int r = 0; r < NRT; ++r) st_rs[16 * (RT0 + r) + (lane & 15)] = acc[r][1][0];
    }
  }
  int ctile[NC];
  ctile[0] = 2 * lst; ctile[1] = 2 * lst + 1;
#pragma unroll
  for (int p = 0; p < NP; ++p) { ctile[2 + p] = 8 + p0 + p; ctile[2 + NP + p] = 14 + p0 + p; }
  if constexpr (MH == 0) {
    if ((lane & 15) == 0) {
#pragma unroll
      for (int ci = 0; ci < NC; ++ci) *(f4*)(st_cs + 16 * ctile[ci] + 4 * (lane >> 4)) = acc[0][ci];
    }
  }
  LBAR();
  f4 cs[NC];
  bf16_t* hp[NP];
#pragma unroll
  for (int p = 0; p < NP; ++p) hp[p] = A2 + a2_off(r0 + (lane & 15), 640 + 96 * cg + 16 * (p0 + p) + 4 * (lane >> 4));
#pragma unroll
  for (int ci = 0; ci < NC; ++ci) cs[ci] = *(const f4*)(st_cs + 16 * ctile[ci] + 4 * (lane >> 4));
  float nm[NRT], sd[NRT], rinv[NRT];
#pragma unroll
  for (int r = 0; r < NRT; ++r) {
    const int row = 16 * (RT0 + r) + (lane & 15);
    const float mean = st_rs[row] * (1.0f / 640.0f);
    const float var = fmaxf(st_sq[row] * (1.0f / 640.0f) - mean * mean, 0.f);
    nm[r] = -bfr(mean);
    sd[r] = bfr(__builtin_amdgcn_sqrtf(bfr(bfr(var) + 1e-5f)));
    rinv[r] = __builtin_amdgcn_rcpf(sd[r]);
  }
#pragma unroll
  for (int r = 0; r < NRT; ++r) {
    const int row = 16 * (RT0 + r) + (lane & 15);
    const f4 nm4 = {nm[r], nm[r], nm[r], nm[r]};
#pragma unroll
    for (int ci = 0; ci < 2; ++ci) {
      f4 y = __builtin_elementwise_fma(cs[ci], nm4, acc[r][ci]) * rinv[r];
      u2 o; o[0] = pk2(y[0], y[1]); o[1] = pk2(y[2], y[3]);
      *(u2*)(lds + row * IMG_STRIDE + (16 * ctile[ci] + 4 * (lane >> 4)) * 2) = o;
    }
  }
  constexpr int R1 = MH == 0 ? 1 : 0;
#pragma unroll
  for (int r = R1; r < NRT; ++r) {
    const f4 nm4 = {nm[r], nm[r], nm[r], nm[r]};
    const float ga = -1.4426950408889634f * rinv[r];
#pragma unroll
    for (int p = 0; p < NP; ++p) {
      acc[r][2 + p] = __builtin_elementwise_fma(cs[2 + p], nm4, acc[r][2 + p]) * ga;
      acc[r][2 + NP + p] = __builtin_elementwise_fma(cs[2 + NP + p], nm4, acc[r][2 + NP + p]);
    }
  }
#pragma unroll
  for (int r = R1; r < NRT; ++r)
#pragma unroll
    for (int p = 0; p < NP; ++p)
#pragma unroll
      for (int e = 0; e < 4; ++e) acc[r][2 + p][e] = __builtin_amdgcn_exp2f(acc[r][2 + p][e]);
#pragma unroll
  for (int r = R1; r < NRT; ++r) {
    const f4 sd4 = {sd[r], sd[r], sd[r], sd[r]};
#pragma unroll
    for (int p = 0; p < NP; ++p) acc[r][2 + p] = __builtin_elementwise_fma(acc[r][2 + p], sd4, sd4);
  }
#pragma unroll
  for (int r = R1; r < NRT; ++r)
#pragma unroll
    for (int p = 0; p < NP; ++p)
#pragma unroll
      for (int e = 0; e < 4; ++e) acc[r][2 + p][e] = __builtin_amdgcn_rcpf(acc[r][2 + p][e]);
#pragma unroll
  for (int r = R1; r < NRT; ++r) {
    const int rt = RT0 + r - 1;
#pragma unroll
    for (int p = 0; p < NP; ++p) {
      f4 d = acc[r][2 + p] * acc[r][2 + NP + p];
      u2 o; o[0] = pk2(d[0], d[1]); o[1] = pk2(d[2], d[3]);
      ST8O(hp[p] + (rt >> 2) * (68 * 4 * 512), o, (rt & 3) * 1024);
    }
  }
}

DEVI float xor1(float v) { return __builtin_bit_cast(float, __builtin_amdgcn_mov_dpp(__builtin_bit_cast(int, v), 0xB1, 0xf, 0xf, true)); }

__global__ __launch_bounds__(512, 2) void k1_kernel(const bf16_t* __restrict__ x, const bf16_t* __restrict__ wqkv,
                                                    const bf16_t* __restrict__ wg, const bf16_t* __restrict__ wu,
                                                    const bf16_t* __restrict__ wo, const bf16_t* __restrict__ wd,
                                                    bf16_t* __restrict__ A2, bf16_t* __restrict__ W2f) {
  __shared__ __attribute__((aligned(16))) char lds[DUMMY_OFF + 1024];
  const int tid = threadIdx.x, lane = tid & 63;
  const int w = __builtin_amdgcn_readfirstlane(tid >> 6);
  const int bid = blockIdx.x, xcd = bid & 7, idx = bid >> 3;
  const int rb = 8 * (xcd & 1) + (idx & 7), cg = 4 * (xcd >> 1) + (idx >> 3);
  const int r0 = rb * 128;
  unsigned wv[2][8];
  const bool two = bid * 8 + w + 2048 < 2720;
#pragma unroll
  for (int t = 0; t < 2; ++t) {
    const int fi = (t == 0 || two) ? bid * 8 + w + 2048 * t : bid * 8 + w;
    const int ks = fi / 40, ct = fi - ks * 40;
    const bf16_t* p = (ks < 20 ? wo + ks * 32 * 640 : wd + (ks - 20) * 32 * 640) + (8 * (lane >> 4)) * 640 + 16 * ct + (lane & 15);
    if (t == 0 || two) {
#pragma unroll
      for (int e = 0; e < 8; ++e)
        asm volatile("global_load_ushort %0, %1, off offset:%2" : "=v"(wv[t][e]) : "v"(p + (e & 4) * 640), "n"((e & 3) * 1280));
    } else {
#pragma unroll
      for (int e = 0; e < 8; ++e) wv[t][e] = 0;
    }
  }
  K1Ctx c;
  {
    const int gw = bid * 8 + w;
    const char* pb = (const char*)g_ones; unsigned per = 0; int id = 0;
    if (gw < 400) { pb = (const char*)wqkv; per = 400; id = gw; }
    else if (gw < 720) { pb = (const char*)wg; per = 320; id = gw - 400; }
    else if (gw < 1040) { pb = (const char*)wu; per = 320; id = gw - 720; }
    else if (gw < 1466) { pb = (const char*)x; per = 426; id = gw - 1040; }
    else if (gw < 1599) { pb = (const char*)wo; per = 133; id = gw - 1466; }
    else if (gw < 1919) { pb = (const char*)wd; per = 320; id = gw - 1599; }
    c.pf = per ? pb + id * 1024 + lane * 16 : pb;
    c.pfs = per * 1024;
  }
#pragma unroll
  for (int i = 0; i < NPF0; ++i) k1_pf(c, lds);
  __builtin_amdgcn_sched_barrier(0);
  {
#pragma unroll
    for (int i = 0; i < 5; ++i) {
      const char* src = (const char*)g_ones; unsigned str = 0, kind = 2, dst = DUMMY_OFF;
      const int pb = w + 8 * i;
      const int pa = i == 2 ? 12 + w : w + 8 * (i - 3);
      if (i < 3 && pb < 20) {
        const unsigned ap = pb * 1024 + lane * 16;
        const unsigned k = ap / 640;
        const unsigned al = ap ^ ((((k >> 1) & 1) | (((k >> 3) & 1) << 1)) << 5);
        const unsigned ch = (al - k * 640) >> 4;
        kind = 0; dst = BOFF + pb * 1024;
        if (ch < 15) {
          src = (const char*)(wqkv + k * 1920 + ((ch * 13) >> 6) * 600 + 40 * cg + 8 * ch); str = 32u * 1920 * 2;
        } else if (ch > 15) {
          src = (const char*)((ch < 28 ? wg + 8 * (ch - 16) : wu + 8 * (ch - 28)) + k * 1536 + 96 * cg); str = 32u * 1536 * 2;
        }
      } else if (i >= 3 || (i == 2 && w < 6)) {
        const int lr = 8 * pa + (lane >> 3);
        int grow = r0 - 16 + lr;
        grow = grow < 0 ? 0 : grow;
        kind = 1; dst = pa * 1024;
        if (lr != 0) { src = (const char*)(x + grow * 640 + (((lane & 7) ^ (lr & 7)) * 8)); str = i == 2 ? 64u : 128u; }
      }
      c.lo[i] = (unsigned)(uint64_t)src; c.hi[i] = (unsigned)((uint64_t)src >> 32); c.str[i] = str;
      c.pdst[i] = dst; c.kind[i] = kind;
    }
  }
#pragma unroll
  for (int s = 0; s < 3; ++s) {
#pragma unroll
    for (int i = 0; i < ((s & 1) ? 3 : 5); ++i) k1_piece(c, lds, i, s * BSLOT, (s >> 1) * ASLOT, true, (s & 1) != 0);
    if (NPF0 + s < 6) k1_pf(c, lds);
    if (s < 2) __builtin_amdgcn_s_barrier();
  }
  asm volatile("s_waitcnt vmcnt(19)"
               : "+v"(wv[0][0]), "+v"(wv[0][1]), "+v"(wv[0][2]), "+v"(wv[0][3]), "+v"(wv[0][4]), "+v"(wv[0][5]), "+v"(wv[0][6]), "+v"(wv[0][7]),
                 "+v"(wv[1][0]), "+v"(wv[1][1]), "+v"(wv[1][2]), "+v"(wv[1][3]), "+v"(wv[1][4]), "+v"(wv[1][5]), "+v"(wv[1][6]), "+v"(wv[1][7])
               :: "memory");
#pragma unroll
  for (int t = 0; t < 2; ++t) {
    if (t == 0 || two) {
      u4 o;
#pragma unroll
      for (int e = 0; e < 4; ++e) o[e] = wv[t][2 * e] | (wv[t][2 * e + 1] << 16);
      ST16(W2f + ((size_t)(bid * 8 + w + 2048 * t) * 64 + lane) * 8, o);
    }
  }
  {
    const int lst = w < 4 ? w : ((w + 2) & 3);
    if (w < 2) k1_main<0, 2>(c, lds, lane, lst, r0, cg, A2);
    else if (w < 4) k1_main<0, 1>(c, lds, lane, lst, r0, cg, A2);
    else if (w < 6) k1_main<1, 1>(c, lds, lane, lst, r0, cg, A2);
    else k1_main<1, 2>(c, lds, lane, lst, r0, cg, A2);
  }
  LBAR();
  {
    const int half = tid & 1, pi = tid >> 1, hh = pi >> 7, row = pi & 127, pos = r0 + row;
    const char* pr = lds + (16 + row) * IMG_STRIDE + 40 * hh;
    const char* pw = pr - 8 * half * IMG_STRIDE;
    unsigned qv[10];
#pragma unroll
    for (int i = 0; i < 5; ++i) { u2 t = *(const u2*)(pr + 8 * i); qv[2 * i] = t[0]; qv[2 * i + 1] = t[1]; }
    float lg[4];
#pragma unroll
    for (int ww = 0; ww < 4; ++ww) {
      const char* pk = pw - 2 * ww * IMG_STRIDE + 80;
      float a = 0.f;
#pragma unroll
      for (int i = 0; i < 5; ++i) {
        u2 t = *(const u2*)(pk + 8 * i);
        a = dot2(qv[2 * i], t[0], a); a = dot2(qv[2 * i + 1], t[1], a);
      }
      a = bfr(bfr(a) * 0.22360679774997896f);
      lg[ww] = (pos - 2 * (4 * half + ww) >= 0) ? a : -1e9f;
    }
    float mx = fmaxf(fmaxf(lg[0], lg[1]), fmaxf(lg[2], lg[3]));
    mx = fmaxf(mx, xor1(mx));
    float sum = 0.f;
#pragma unroll
    for (int ww = 0; ww < 4; ++ww) { lg[ww] = __expf(lg[ww] - mx); sum += lg[ww]; }
    sum += xor1(sum);
    const float inv = __builtin_amdgcn_rcpf(sum);
    float o[20];
#pragma unroll
    for (int i = 0; i < 20; ++i) o[i] = 0.f;
#pragma unroll
    for (int wp = 0; wp < 2; ++wp) {
      const unsigned P = pk2(lg[2 * wp] * inv, lg[2 * wp + 1] * inv);
      const char* pv0 = pw - 4 * wp * IMG_STRIDE + 160;
      const char* pv1 = pv0 - 2 * IMG_STRIDE;
#pragma unroll
      for (int i = 0; i < 5; ++i) {
        u2 a = *(const u2*)(pv0 + 8 * i), b = *(const u2*)(pv1 + 8 * i);
#pragma unroll
        for (int e = 0; e < 2; ++e) {
          unsigned lo = __builtin_amdgcn_perm(b[e], a[e], 0x05040100u);
          unsigned hi = __builtin_amdgcn_perm(b[e], a[e], 0x07060302u);
          o[4 * i + 2 * e] = dot2(lo, P, o[4 * i + 2 * e]);
          o[4 * i + 2 * e + 1] = dot2(hi, P, o[4 * i + 2 * e + 1]);
        }
      }
    }
#pragma unroll
    for (int i = 0; i < 20; ++i) o[i] += xor1(o[i]);
    const int kc0 = 40 * cg + 20 * hh;
    unsigned pk[10];
#pragma unroll
    for (int i = 0; i < 10; ++i) pk[i] = pk2(o[2 * i], o[2 * i + 1]);
    const unsigned mh = 0u - (unsigned)hh, mf = 0u - (unsigned)half;
    u4 cv; u2 pv;
#pragma unroll
    for (int e = 0; e < 4; ++e) {
      const unsigned lo = pk[e] ^ ((pk[e] ^ pk[e + 2]) & mh), hi = pk[4 + e] ^ ((pk[4 + e] ^ pk[6 + e]) & mh);
      cv[e] = lo ^ ((lo ^ hi) & mf);
    }
    pv[0] = pk[8] ^ ((pk[8] ^ pk[0]) & mh);
    pv[1] = pk[9] ^ ((pk[9] ^ pk[1]) & mh);
    ST16(A2 + a2_off(pos, kc0 + 4 * hh + 8 * half), cv);
    if (half == 0) ST8(A2 + a2_off(pos, kc0 + 16 * (1 - hh)), pv);
  }
}


__global__ __launch_bounds__(256, 2) void k2_kernel(const bf16_t* __restrict__ A2, const bf16_t* __restrict__ W2f,
                                                    const bf16_t* __restrict__ x, bf16_t* __restrict__ out) {
  __shared__ __attribute__((aligned(16))) char lds[4 * 15 * 1024];
  const int tid = threadIdx.x, lane = tid & 63;
  const int w = __builtin_amdgcn_readfirstlane(tid >> 6);
  const int bid = blockIdx.x, xcd = bid & 7, idx = bid >> 3;
  const int rb64 = 8 * (xcd & 3) + (idx & 7), cb = 4 * (xcd >> 2) + (idx >> 3);
  f4 acc[4][5];
#pragma unroll
  for (int r = 0; r < 4; ++r)
#pragma unroll
    for (int q = 0; q < 5; ++q) acc[r][q] = f4{0.f, 0.f, 0.f, 0.f};
  const char* ap[4];
#pragma unroll
  for (int r = 0; r < 4; ++r) ap[r] = (const char*)(A2 + ((size_t)((rb64 * 68 + w) * 4 + ((r + w) & 3)) * 64 + lane) * 8);
  const char* bp = (const char*)(W2f + ((size_t)(w * 40 + 5 * cb) * 64 + lane) * 8);
  u4 fa[2][4], fb[2][5];
  auto issue = [&](int b) {
#pragma unroll
    for (int r = 0; r < 4; ++r) {
      asm volatile("global_load_dwordx4 %0, %1, off" : "=v"(fa[b][r]) : "v"(ap[r]));
      ap[r] += 4 * 4096;
    }
#pragma unroll
    for (int q = 0; q < 4; ++q)
      asm volatile("global_load_dwordx4 %0, %1, off offset:%2" : "=v"(fb[b][q]) : "v"(bp), "n"(q * 1024 - 2048));
    asm volatile("global_load_dwordx4 %0, %1, off offset:2048" : "=v"(fb[b][4]) : "v"(bp));
    bp += 4 * 40 * 1024;
  };
#define K2_WAIT(b, n) asm volatile("s_waitcnt vmcnt(" #n ")" : "+v"(fa[b][0]), "+v"(fa[b][1]), "+v"(fa[b][2]), "+v"(fa[b][3]), \
                 "+v"(fb[b][0]), "+v"(fb[b][1]), "+v"(fb[b][2]), "+v"(fb[b][3]), "+v"(fb[b][4]) :: "memory")
  auto mm = [&](int b) {
#pragma unroll
    for (int r = 3; r >= 0; --r)
#pragma unroll
      for (int ci = 0; ci < 5; ++ci) acc[r][ci] = MFMA(asbf(fb[b][ci]), asbf(fa[b][r]), acc[r][ci]);
  };
  const int grow = rb64 * 64 + 16 * w + (lane & 15), gcol = 80 * cb + 4 * (lane >> 4);
  u2 xv[5];
  bp += 2048;
  issue(0);
#pragma unroll 1
  for (int it = 0; it < 8; ++it) {
    issue(1);
    K2_WAIT(0, 9);
    mm(0);
    issue(0);
    K2_WAIT(1, 9);
    mm(1);
  }
  {
    const bf16_t* xp = x + grow * 640 + gcol;
#pragma unroll
    for (int ci = 0; ci < 5; ++ci)
      asm volatile("global_load_dwordx2 %0, %1, off offset:%2" : "=v"(xv[ci]) : "v"(xp), "n"(ci * 32));
  }
  K2_WAIT(0, 5);
  mm(0);
#pragma unroll
  for (int r = 3; r >= 1; --r) {
    char* d = lds + ((((r + w) & 3) * 3 + r - 1) * 5) * 1024 + lane * 16;
#pragma unroll
    for (int q = 0; q < 5; ++q) *(f4*)(d + q * 1024) = acc[r][q];
  }
  LBAR();
  asm volatile("s_waitcnt vmcnt(0)" : "+v"(xv[0]), "+v"(xv[1]), "+v"(xv[2]), "+v"(xv[3]), "+v"(xv[4]) :: "memory");
#pragma unroll
  for (int q = 0; q < 5; ++q) {
    f4 s = acc[0][q];
#pragma unroll
    for (int t = 0; t < 3; ++t) s += *(const f4*)(lds + ((w * 3 + t) * 5 + q) * 1024 + lane * 16);
    u2 o;
    o[0] = pk2(s[0] + lo16(xv[q][0]), s[1] + hi16(xv[q][0]));
    o[1] = pk2(s[2] + lo16(xv[q][1]), s[3] + hi16(xv[q][1]));
    *(u2*)(out + grow * 640 + gcol + 16 * q) = o;
  }
}

extern "C" void kernel_launch(void* const* d_in, const int* in_sizes, int n_in, void* d_out, int out_size,
                              void* d_ws, size_t ws_size, hipStream_t stream) {
  const bf16_t* x = (const bf16_t*)d_in[0];
  const bf16_t* wqkv = (const bf16_t*)d_in[1];
  const bf16_t* wo = (const bf16_t*)d_in[2];
  const bf16_t* wg = (const bf16_t*)d_in[4];
  const bf16_t* wu = (const bf16_t*)d_in[5];
  const bf16_t* wd = (const bf16_t*)d_in[6];
  bf16_t* A2 = (bf16_t*)d_ws;
  bf16_t* W2f = A2 + 6 * 1024 * 1024;
  k1_kernel<<<256, 512, 0, stream>>>(x, wqkv, wg, wu, wo, wd, A2, W2f);
  k2_kernel<<<256, 256, 0, stream>>>(A2, W2f, x, (bf16_t*)d_out);
}
```

```cpp
#include <hip/hip_runtime.h>
#include <hip/hip_bf16.h>
#include <stdint.h>

typedef __bf16 bf8 __attribute__((ext_vector_type(8)));
typedef __bf16 bf2 __attribute__((ext_vector_type(2)));
typedef float f4 __attribute__((ext_vector_type(4)));
typedef unsigned u4 __attribute__((ext_vector_type(4)));
typedef unsigned u2 __attribute__((ext_vector_type(2)));
typedef unsigned short bf16_t;

#define LAS __attribute__((address_space(3)))
#define GAS __attribute__((address_space(1)))
#define DEVI __device__ __forceinline__

__device__ const __attribute__((aligned(16))) unsigned g_ones[4] = {0x3F803F80u, 0x3F803F80u, 0x3F803F80u, 0x3F803F80u};

DEVI float bfr(float v) { return (float)(__bf16)v; }
DEVI unsigned pk2(float a, float b) { bf2 c; c[0] = (__bf16)a; c[1] = (__bf16)b; return __builtin_bit_cast(unsigned, c); }
DEVI float lo16(unsigned v) { return __builtin_bit_cast(float, v << 16); }
DEVI float hi16(unsigned v) { return __builtin_bit_cast(float, v & 0xffff0000u); }
DEVI float dot2(unsigned a, unsigned b, float c) {
  return __builtin_amdgcn_fdot2_f32_bf16(__builtin_bit_cast(bf2, a), __builtin_bit_cast(bf2, b), c, false);
}
DEVI void glds16(const void* g, unsigned ldsoff, char* lds) {
  __builtin_amdgcn_global_load_lds((const GAS void*)g, (LAS void*)(lds + ldsoff), 16, 0, 0);
}
#define RD128(d, addr, off) asm volatile("ds_read_b128 %0, %1 offset:%2" : "=v"(d) : "v"(addr), "n"(off))
#define RDTR(d, addr, off) asm volatile("ds_read_b64_tr_b16 %0, %1 offset:%2" : "=v"(d) : "v"(addr), "n"(off))
DEVI bf8 cat2(u2 a, u2 b) { u4 r; r[0] = a[0]; r[1] = a[1]; r[2] = b[0]; r[3] = b[1]; return __builtin_bit_cast(bf8, r); }
DEVI bf8 asbf(u4 a) { return __builtin_bit_cast(bf8, a); }
#define MFMA(a, b, c) __builtin_amdgcn_mfma_f32_16x16x32_bf16(a, b, c, 0, 0, 0)
#define WAITV(n) asm volatile("s_waitcnt vmcnt(" #n ")" ::: "memory")

#define ST8(p, v) asm volatile("global_store_dwordx2 %0, %1, off sc0 sc1" :: "v"(p), "v"(v) : "memory")
#define ST8O(p, v, o) asm volatile("global_store_dwordx2 %0, %1, off offset:%2 sc0 sc1" :: "v"(p), "v"(v), "n"(o) : "memory")
#define ST16(p, v) asm volatile("global_store_dwordx4 %0, %1, off sc0 sc1\n\ts_nop 1" :: "v"(p), "v"(v) : "memory")
constexpr int GM = 4;
constexpr int NPF0 = 3;
constexpr int ASLOT = 18432;
constexpr int BSLOT = 20480;
constexpr int BOFF = 3 * ASLOT;
constexpr int DUMMY_OFF = BOFF + 5 * BSLOT;
constexpr int IMG_STRIDE = 264;
constexpr int STAT_OFF = 144 * IMG_STRIDE;

DEVI unsigned a2_off(int grow, int kcol) {
  return (((((unsigned)grow >> 6) * 68 + ((unsigned)kcol >> 5)) * 4 + (((unsigned)grow >> 4) & 3)) * 64 +
          (((((unsigned)kcol >> 3) & 3) << 4) | ((unsigned)grow & 15))) * 8 + ((unsigned)kcol & 7);
}

struct K1Ctx {
  unsigned lo[5], hi[5];
  unsigned str[5];
  unsigned pdst[5];
  unsigned kind[5];
  const char* pf;
  unsigned pfs;
};
DEVI void k1_pf(K1Ctx& c, char* lds) {
  glds16(c.pf, DUMMY_OFF, lds);
  c.pf += c.pfs;
}

DEVI void k1_piece(K1Ctx& c, char* lds, int i, unsigned sb, unsigned sa, bool live, bool oddg) {
  const bool lv = (i == 2 && oddg) ? (live && c.kind[2] == 0) : live;
  const char* p = lv ? (const char*)(((uint64_t)c.hi[i] << 32) | c.lo[i]) : (const char*)g_ones;
  unsigned d;
  if (i < 2) d = sb + c.pdst[i];
  else if (i > 2) d = sa + c.pdst[i];
  else if (oddg) d = c.kind[2] == 0 ? sb + c.pdst[2] : (unsigned)DUMMY_OFF;
  else d = (c.kind[2] == 0 ? sb : c.kind[2] == 1 ? sa : 0u) + c.pdst[2];
  glds16(p, d, lds);
  asm("v_add_co_u32 %0, vcc, %0, %2\n\tv_addc_co_u32 %1, vcc, 0, %1, vcc" : "+v"(c.lo[i]), "+v"(c.hi[i]) : "v"(c.str[i]) : "vcc");
}
#define LBAR() do { asm volatile("s_waitcnt lgkmcnt(0)" ::: "memory"); __builtin_amdgcn_s_barrier(); asm volatile("" ::: "memory"); } while (0)

template <int MH, int NP>
DEVI void k1_main(K1Ctx& c, char* lds, int lane, int lst, int r0, int cg, bf16_t* __restrict__ A2) {
  constexpr int NRT = MH == 0 ? 5 : 4;
  constexpr int RT0 = MH == 0 ? 0 : 5;
  constexpr int NC = 2 + 2 * NP;
  constexpr int NG = NP == 2 ? 0 : (MH == 0 ? 3 : 2);
  constexpr int NA = NRT + NG;
  f4 acc[NRT][NC];
  f4 gacc[NG + 1];
#pragma unroll
  for (int r = 0; r < NRT; ++r)
#pragma unroll
    for (int q = 0; q < NC; ++q) acc[r][q] = f4{0.f, 0.f, 0.f, 0.f};
#pragma unroll
  for (int r = 0; r < NG + 1; ++r) gacc[r] = f4{0.f, 0.f, 0.f, 0.f};
  const unsigned lbase = (unsigned)(uintptr_t)(LAS char*)lds;
  const int ar = lane & 15, ac = lane >> 4;
  const unsigned abase0 = lbase + ar * 128 + ((ac ^ (ar & 7)) * 16);
  const unsigned abase1 = lbase + ar * 128 + (((4 + ac) ^ (ar & 7)) * 16);
  const int gt0 = MH == 0 ? 3 * (lst - 2) : 6 + (lst - 2);
  const int g = lane >> 4, q4 = (lane & 15) >> 2, p4 = lane & 3;
  const int p0 = NP == 2 ? 2 * lst : lst + 2;
  const unsigned hx = (((q4 >> 1) & 1) | ((g & 1) << 1)) << 5;
  auto baddr = [&](int ct) { return lbase + BOFF + ((((8 * g + q4) * 640 + 32 * ct) ^ hx) + 8 * p4); };
  unsigned bb[NC];
  bb[0] = baddr(2 * lst); bb[1] = baddr(2 * lst + 1);
#pragma unroll
  for (int p = 0; p < NP; ++p) { bb[2 + p] = baddr(8 + p0 + p); bb[2 + NP + p] = baddr(14 + p0 + p); }
  u4 fa[2][NA];
  u2 fb[2][NC][2];
  unsigned a0, g0, bc[NC];
  auto bases = [&](int odd, unsigned sb, unsigned sa) {
    a0 = (odd ? abase1 : abase0) + sa; g0 = a0 + gt0 * 2048;
#pragma unroll
    for (int ci = 0; ci < NC; ++ci) bc[ci] = bb[ci] + sb;
  };
  auto memop = [&](int nb, int k, int npc, unsigned sb, unsigned sa, bool live, bool liveA) {
    if (k < 2 * NC) {
      RDTR(fb[nb][k >> 1][k & 1], bc[k >> 1], (k & 1) * 2560);
    } else if (k < 2 * NC + NA) {
      const int r = k - 2 * NC;
      if (r < NRT) RD128(fa[nb][r], a0, (RT0 + r) * 2048);
      else RD128(fa[nb][r], g0, (r - NRT) * 2048);
    } else if (k < 2 * NC + NA + npc) {
      const int i = k - 2 * NC - NA;
      k1_piece(c, lds, i, sb, sa, live, npc == 3);
    }
  };
  auto wait_frags = [&](int buf) {
    asm volatile("s_waitcnt lgkmcnt(0)"
                 : "+v"(fa[buf][0]), "+v"(fa[buf][1]), "+v"(fa[buf][2]), "+v"(fa[buf][3]),
                   "+v"(fb[buf][0][0]), "+v"(fb[buf][0][1]), "+v"(fb[buf][1][0]), "+v"(fb[buf][1][1]), "+v"(fb[buf][2][0]),
                   "+v"(fb[buf][2][1]), "+v"(fb[buf][3][0]), "+v"(fb[buf][3][1])
                 :: "memory");
#pragma unroll
    for (int r = 4; r < NA; ++r) asm volatile("" : "+v"(fa[buf][r]) :: "memory");
#pragma unroll
    for (int q = 4; q < NC; ++q) asm volatile("" : "+v"(fb[buf][q][0]), "+v"(fb[buf][q][1]) :: "memory");
  };
  WAITV(8);
  __builtin_amdgcn_s_barrier();
  bases(0, 0, 0);
#pragma unroll
  for (int k = 0; k < 2 * NC + NA; ++k) memop(0, k, 0, 0, 0, true, true);
  unsigned sw = 0;
  unsigned sa = 0;
  auto step = [&](int b, bool live, unsigned san) {
    const unsigned sn = (sw == 4 * BSLOT) ? 0u : sw + BSLOT;
    const unsigned sap = (sa == 0) ? 2 * ASLOT : sa - ASLOT;
    const unsigned sp = sw >= 2 * BSLOT ? sw - 2 * BSLOT : sw + 3 * BSLOT;
    bases(b ^ 1, sn, b ? san : sa);
    if (b == 0) WAITV(5); else WAITV(3);
    wait_frags(b);
    __builtin_amdgcn_s_barrier();
    const int npc = b ? 5 : 3;
    auto mo = [&](int k) {
      if ((k % GM) == GM - 1 && (k / GM) < npc) memop(b ^ 1, 2 * NC + NA + (k / GM), npc, sp, sap, live, live);
      else { const int kr = k - ((k / GM) < npc ? (k / GM) : npc); if (kr < 2 * NC + NA) memop(b ^ 1, kr, npc, sp, sap, live, live); }
    };
    int k = 0;
#pragma unroll
    for (int ci = 0; ci < NC; ++ci)
#pragma unroll
      for (int r = 0; r < NRT; ++r) {
        acc[r][ci] = MFMA(cat2(fb[b][ci][0], fb[b][ci][1]), asbf(fa[b][r]), acc[r][ci]);
        mo(k); ++k;
        __builtin_amdgcn_sched_barrier(0);
      }
#pragma unroll
    for (int r = 0; r < NG; ++r) {
      gacc[r] = MFMA(asbf(fa[b][NRT + r]), asbf(fa[b][NRT + r]), gacc[r]);
      mo(k); ++k;
      __builtin_amdgcn_sched_barrier(0);
    }
#pragma unroll
    for (int kk = NC * NRT + NG; kk < 2 * NC + NA + 8; ++kk) mo(kk);
    sw = sn;
  };
#pragma unroll 1
  for (int s = 0; s < 20; s += 2) {
    const unsigned san = (sa == 2 * ASLOT) ? 0u : sa + ASLOT;
    step(0, s < 17, san);
    step(1, s + 1 < 17, san);
    sa = san;
  }
  asm volatile("s_waitcnt vmcnt(0) lgkmcnt(0)" ::: "memory");
  __builtin_amdgcn_s_barrier();
  float* st_rs = (float*)(lds + STAT_OFF);
  float* st_sq = st_rs + 144;
  float* st_cs = st_sq + 144;
  if constexpr (NG > 0) {
    const int rr = lane & 3;
    if (((lane & 15) >> 2) == (lane >> 4)) {
#pragma unroll
      for (int r = 0; r < NG; ++r)
        st_sq[16 * (gt0 + r) + (lane & 15)] = rr == 0 ? gacc[r][0] : rr == 1 ? gacc[r][1] : rr == 2 ? gacc[r][2] : gacc[r][3];
    }
    if (lst == 3 && (lane >> 4) == 2) {
#pragma unroll
      for (int r = 0; r < NRT; ++r) st_rs[16 * (RT0 + r) + (lane & 15)] = acc[r][1][0];
    }
  }
  int ctile[NC];
  ctile[0] = 2 * lst; ctile[1] = 2 * lst + 1;
#pragma unroll
  for (int p = 0; p < NP; ++p) { ctile[2 + p] = 8 + p0 + p; ctile[2 + NP + p] = 14 + p0 + p; }
  if constexpr (MH == 0) {
    if ((lane & 15) == 0) {
#pragma unroll
      for (int ci = 0; ci < NC; ++ci) *(f4*)(st_cs + 16 * ctile[ci] + 4 * (lane >> 4)) = acc[0][ci];
    }
  }
  LBAR();
  f4 cs[NC];
  bf16_t* hp[NP];
#pragma unroll
  for (int p = 0; p < NP; ++p) hp[p] = A2 + a2_off(r0 + (lane & 15), 640 + 96 * cg + 16 * (p0 + p) + 4 * (lane >> 4));
#pragma unroll
  for (int ci = 0; ci < NC; ++ci) cs[ci] = *(const f4*)(st_cs + 16 * ctile[ci] + 4 * (lane >> 4));
  float nm[NRT], sd[NRT], rinv[NRT];
#pragma unroll
  for (int r = 0; r < NRT; ++r) {
    const int row = 16 * (RT0 + r) + (lane & 15);
    const float mean = st_rs[row] * (1.0f / 640.0f);
    const float var = fmaxf(st_sq[row] * (1.0f / 640.0f) - mean * mean, 0.f);
    nm[r] = -bfr(mean);
    sd[r] = bfr(__builtin_amdgcn_sqrtf(bfr(bfr(var) + 1e-5f)));
    rinv[r] = __builtin_amdgcn_rcpf(sd[r]);
  }
#pragma unroll
  for (int r = 0; r < NRT; ++r) {
    const int row = 16 * (RT0 + r) + (lane & 15);
    const f4 nm4 = {nm[r], nm[r], nm[r], nm[r]};
#pragma unroll
    for (int ci = 0; ci < 2; ++ci) {
      f4 y = __builtin_elementwise_fma(cs[ci], nm4, acc[r][ci]) * rinv[r];
      u2 o; o[0] = pk2(y[0], y[1]); o[1] = pk2(y[2], y[3]);
      *(u2*)(lds + row * IMG_STRIDE + (16 * ctile[ci] + 4 * (lane >> 4)) * 2) = o;
    }
  }
  constexpr int R1 = MH == 0 ? 1 : 0;
#pragma unroll
  for (int r = R1; r < NRT; ++r) {
    const f4 nm4 = {nm[r], nm[r], nm[r], nm[r]};
    const float ga = -1.4426950408889634f * rinv[r];
#pragma unroll
    for (int p = 0; p < NP; ++p) {
      acc[r][2 + p] = __builtin_elementwise_fma(cs[2 + p], nm4, acc[r][2 + p]) * ga;
      acc[r][2 + NP + p] = __builtin_elementwise_fma(cs[2 + NP + p], nm4, acc[r][2 + NP + p]);
    }
  }
#pragma unroll
  for (int r = R1; r < NRT; ++r)
#pragma unroll
    for (int p = 0; p < NP; ++p)
#pragma unroll
      for (int e = 0; e < 4; ++e) acc[r][2 + p][e] = __builtin_amdgcn_exp2f(acc[r][2 + p][e]);
#pragma unroll
  for (int r = R1; r < NRT; ++r) {
    const f4 sd4 = {sd[r], sd[r], sd[r], sd[r]};
#pragma unroll
    for (int p = 0; p < NP; ++p) acc[r][2 + p] = __builtin_elementwise_fma(acc[r][2 + p], sd4, sd4);
  }
#pragma unroll
  for (int r = R1; r < NRT; ++r)
#pragma unroll
    for (int p = 0; p < NP; ++p)
#pragma unroll
      for (int e = 0; e < 4; ++e) acc[r][2 + p][e] = __builtin_amdgcn_rcpf(acc[r][2 + p][e]);
#pragma unroll
  for (int r = R1; r < NRT; ++r) {
    const int rt = RT0 + r - 1;
#pragma unroll
    for (int p = 0; p < NP; ++p) {
      f4 d = acc[r][2 + p] * acc[r][2 + NP + p];
      u2 o; o[0] = pk2(d[0], d[1]); o[1] = pk2(d[2], d[3]);
      ST8O(hp[p] + (rt >> 2) * (68 * 4 * 512), o, (rt & 3) * 1024);
    }
  }
}

DEVI float xor1(float v) { return __builtin_bit_cast(float, __builtin_amdgcn_mov_dpp(__builtin_bit_cast(int, v), 0xB1, 0xf, 0xf, true)); }

__global__ __launch_bounds__(512, 2) void k1_kernel(const bf16_t* __restrict__ x, const bf16_t* __restrict__ wqkv,
                                                    const bf16_t* __restrict__ wg, const bf16_t* __restrict__ wu,
                                                    const bf16_t* __restrict__ wo, const bf16_t* __restrict__ wd,
                                                    bf16_t* __restrict__ A2, bf16_t* __restrict__ W2f) {
  __shared__ __attribute__((aligned(16))) char lds[DUMMY_OFF + 1024];
  const int tid = threadIdx.x, lane = tid & 63;
  const int w = __builtin_amdgcn_readfirstlane(tid >> 6);
  const int bid = blockIdx.x, xcd = bid & 7, idx = bid >> 3;
  const int rb = 8 * (xcd & 1) + (idx & 7), cg = 4 * (xcd >> 1) + (idx >> 3);
  const int r0 = rb * 128;
  unsigned wv[2][8];
  const bool two = bid * 8 + w + 2048 < 2720;
#pragma unroll
  for (int t = 0; t < 2; ++t) {
    const int fi = (t == 0 || two) ? bid * 8 + w + 2048 * t : bid * 8 + w;
    const int ks = fi / 40, ct = fi - ks * 40;
    const bf16_t* p = (ks < 20 ? wo + ks * 32 * 640 : wd + (ks - 20) * 32 * 640) + (8 * (lane >> 4)) * 640 + 16 * ct + (lane & 15);
    if (t == 0 || two) {
#pragma unroll
      for (int e = 0; e < 8; ++e)
        asm volatile("global_load_ushort %0, %1, off offset:%2" : "=v"(wv[t][e]) : "v"(p + (e & 4) * 640), "n"((e & 3) * 1280));
    } else {
#pragma unroll
      for (int e = 0; e < 8; ++e) wv[t][e] = 0;
    }
  }
  K1Ctx c;
  {
    const int gw = bid * 8 + w;
    const char* pb; unsigned per; int id;
    if (gw < 788) { pb = (const char*)wqkv; per = 788; id = gw; }
    else if (gw < 1418) { pb = (const char*)wg; per = 630; id = gw - 788; }
    else { pb = (const char*)wu; per = 630; id = gw - 1418; }
    c.pf = pb + id * 1024 + lane * 16;
    c.pfs = per * 1024;
  }
#pragma unroll
  for (int i = 0; i < NPF0; ++i) k1_pf(c, lds);
  __builtin_amdgcn_sched_barrier(0);
  {
#pragma unroll
    for (int i = 0; i < 5; ++i) {
      const char* src = (const char*)g_ones; unsigned str = 0, kind = 2, dst = DUMMY_OFF;
      const int pb = w + 8 * i;
      const int pa = i == 2 ? 12 + w : w + 8 * (i - 3);
      if (i < 3 && pb < 20) {
        const unsigned ap = pb * 1024 + lane * 16;
        const unsigned k = ap / 640;
        const unsigned al = ap ^ ((((k >> 1) & 1) | (((k >> 3) & 1) << 1)) << 5);
        const unsigned ch = (al - k * 640) >> 4;
        kind = 0; dst = BOFF + pb * 1024;
        if (ch < 15) {
          src = (const char*)(wqkv + k * 1920 + ((ch * 13) >> 6) * 600 + 40 * cg + 8 * ch); str = 32u * 1920 * 2;
        } else if (ch > 15) {
          src = (const char*)((ch < 28 ? wg + 8 * (ch - 16) : wu + 8 * (ch - 28)) + k * 1536 + 96 * cg); str = 32u * 1536 * 2;
        }
      } else if (i >= 3 || (i == 2 && w < 6)) {
        const int lr = 8 * pa + (lane >> 3);
        int grow = r0 - 16 + lr;
        grow = grow < 0 ? 0 : grow;
        kind = 1; dst = pa * 1024;
        if (lr != 0) { src = (const char*)(x + grow * 640 + (((lane & 7) ^ (lr & 7)) * 8)); str = i == 2 ? 64u : 128u; }
      }
      c.lo[i] = (unsigned)(uint64_t)src; c.hi[i] = (unsigned)((uint64_t)src >> 32); c.str[i] = str;
      c.pdst[i] = dst; c.kind[i] = kind;
    }
  }
#pragma unroll
  for (int s = 0; s < 3; ++s) {
#pragma unroll
    for (int i = 0; i < ((s & 1) ? 3 : 5); ++i) k1_piece(c, lds, i, s * BSLOT, (s >> 1) * ASLOT, true, (s & 1) != 0);
    if (s < 2) __builtin_amdgcn_s_barrier();
  }
  asm volatile("s_waitcnt vmcnt(16)"
               : "+v"(wv[0][0]), "+v"(wv[0][1]), "+v"(wv[0][2]), "+v"(wv[0][3]), "+v"(wv[0][4]), "+v"(wv[0][5]), "+v"(wv[0][6]), "+v"(wv[0][7]),
                 "+v"(wv[1][0]), "+v"(wv[1][1]), "+v"(wv[1][2]), "+v"(wv[1][3]), "+v"(wv[1][4]), "+v"(wv[1][5]), "+v"(wv[1][6]), "+v"(wv[1][7])
               :: "memory");
#pragma unroll
  for (int t = 0; t < 2; ++t) {
    if (t == 0 || two) {
      u4 o;
#pragma unroll
      for (int e = 0; e < 4; ++e) o[e] = wv[t][2 * e] | (wv[t][2 * e + 1] << 16);
      ST16(W2f + ((size_t)(bid * 8 + w + 2048 * t) * 64 + lane) * 8, o);
    }
  }
  {
    const int lst = w < 4 ? w : ((w + 2) & 3);
    if (w < 2) k1_main<0, 2>(c, lds, lane, lst, r0, cg, A2);
    else if (w < 4) k1_main<0, 1>(c, lds, lane, lst, r0, cg, A2);
    else if (w < 6) k1_main<1, 1>(c, lds, lane, lst, r0, cg, A2);
    else k1_main<1, 2>(c, lds, lane, lst, r0, cg, A2);
  }
  LBAR();
  {
    const int half = tid & 1, pi = tid >> 1, hh = pi >> 7, row = pi & 127, pos = r0 + row;
    const char* pr = lds + (16 + row) * IMG_STRIDE + 40 * hh;
    const char* pw = pr - 8 * half * IMG_STRIDE;
    unsigned qv[10];
#pragma unroll
    for (int i = 0; i < 5; ++i) { u2 t = *(const u2*)(pr + 8 * i); qv[2 * i] = t[0]; qv[2 * i + 1] = t[1]; }
    float lg[4];
#pragma unroll
    for (int ww = 0; ww < 4; ++ww) {
      const char* pk = pw - 2 * ww * IMG_STRIDE + 80;
      float a = 0.f;
#pragma unroll
      for (int i = 0; i < 5; ++i) {
        u2 t = *(const u2*)(pk + 8 * i);
        a = dot2(qv[2 * i], t[0], a); a = dot2(qv[2 * i + 1], t[1], a);
      }
      a = bfr(bfr(a) * 0.22360679774997896f);
      lg[ww] = (pos - 2 * (4 * half + ww) >= 0) ? a : -1e9f;
    }
    float mx = fmaxf(fmaxf(lg[0], lg[1]), fmaxf(lg[2], lg[3]));
    mx = fmaxf(mx, xor1(mx));
    float sum = 0.f;
#pragma unroll
    for (int ww = 0; ww < 4; ++ww) { lg[ww] = __expf(lg[ww] - mx); sum += lg[ww]; }
    sum += xor1(sum);
    const float inv = __builtin_amdgcn_rcpf(sum);
    float o[20];
#pragma unroll
    for (int i = 0; i < 20; ++i) o[i] = 0.f;
#pragma unroll
    for (int wp = 0; wp < 2; ++wp) {
      const unsigned P = pk2(lg[2 * wp] * inv, lg[2 * wp + 1] * inv);
      const char* pv0 = pw - 4 * wp * IMG_STRIDE + 160;
      const char* pv1 = pv0 - 2 * IMG_STRIDE;
#pragma unroll
      for (int i = 0; i < 5; ++i) {
        u2 a = *(const u2*)(pv0 + 8 * i), b = *(const u2*)(pv1 + 8 * i);
#pragma unroll
        for (int e = 0; e < 2; ++e) {
          unsigned lo = __builtin_amdgcn_perm(b[e], a[e], 0x05040100u);
          unsigned hi = __builtin_amdgcn_perm(b[e], a[e], 0x07060302u);
          o[4 * i + 2 * e] = dot2(lo, P, o[4 * i + 2 * e]);
          o[4 * i + 2 * e + 1] = dot2(hi, P, o[4 * i + 2 * e + 1]);
        }
      }
    }
#pragma unroll
    for (int i = 0; i < 20; ++i) o[i] += xor1(o[i]);
    const int kc0 = 40 * cg + 20 * hh;
    unsigned pk[10];
#pragma unroll
    for (int i = 0; i < 10; ++i) pk[i] = pk2(o[2 * i], o[2 * i + 1]);
    const unsigned mh = 0u - (unsigned)hh, mf = 0u - (unsigned)half;
    u4 cv; u2 pv;
#pragma unroll
    for (int e = 0; e < 4; ++e) {
      const unsigned lo = pk[e] ^ ((pk[e] ^ pk[e + 2]) & mh), hi = pk[4 + e] ^ ((pk[4 + e] ^ pk[6 + e]) & mh);
      cv[e] = lo ^ ((lo ^ hi) & mf);
    }
    pv[0] = pk[8] ^ ((pk[8] ^ pk[0]) & mh);
    pv[1] = pk[9] ^ ((pk[9] ^ pk[1]) & mh);
    ST16(A2 + a2_off(pos, kc0 + 4 * hh + 8 * half), cv);
    if (half == 0) ST8(A2 + a2_off(pos, kc0 + 16 * (1 - hh)), pv);
  }
}


__global__ __launch_bounds__(256, 2) void k2_kernel(const bf16_t* __restrict__ A2, const bf16_t* __restrict__ W2f,
                                                    const bf16_t* __restrict__ x, bf16_t* __restrict__ out) {
  __shared__ __attribute__((aligned(16))) char lds[4 * 15 * 1024];
  const int tid = threadIdx.x, lane = tid & 63;
  const int w = __builtin_amdgcn_readfirstlane(tid >> 6);
  const int bid = blockIdx.x, xcd = bid & 7, idx = bid >> 3;
  const int rb64 = 8 * (xcd & 3) + (idx & 7), cb = 4 * (xcd >> 2) + (idx >> 3);
  f4 acc[4][5];
#pragma unroll
  for (int r = 0; r < 4; ++r)
#pragma unroll
    for (int q = 0; q < 5; ++q) acc[r][q] = f4{0.f, 0.f, 0.f, 0.f};
  const char* ap[4];
#pragma unroll
  for (int r = 0; r < 4; ++r) ap[r] = (const char*)(A2 + ((size_t)((rb64 * 68 + w) * 4 + ((r + w) & 3)) * 64 + lane) * 8);
  const char* bp = (const char*)(W2f + ((size_t)(w * 40 + 5 * cb) * 64 + lane) * 8);
  u4 fa[2][4], fb[2][5];
  auto issue = [&](int b) {
#pragma unroll
    for (int r = 0; r < 4; ++r) {
      asm volatile("global_load_dwordx4 %0, %1, off" : "=v"(fa[b][r]) : "v"(ap[r]));
      ap[r] += 4 * 4096;
    }
#pragma unroll
    for (int q = 0; q < 4; ++q)
      asm volatile("global_load_dwordx4 %0, %1, off offset:%2" : "=v"(fb[b][q]) : "v"(bp), "n"(q * 1024 - 2048));
    asm volatile("global_load_dwordx4 %0, %1, off offset:2048" : "=v"(fb[b][4]) : "v"(bp));
    bp += 4 * 40 * 1024;
  };
#define K2_WAIT(b, n) asm volatile("s_waitcnt vmcnt(" #n ")" : "+v"(fa[b][0]), "+v"(fa[b][1]), "+v"(fa[b][2]), "+v"(fa[b][3]), \
                 "+v"(fb[b][0]), "+v"(fb[b][1]), "+v"(fb[b][2]), "+v"(fb[b][3]), "+v"(fb[b][4]) :: "memory")
  auto mm = [&](int b) {
#pragma unroll
    for (int r = 3; r >= 0; --r)
#pragma unroll
      for (int ci = 0; ci < 5; ++ci) acc[r][ci] = MFMA(asbf(fb[b][ci]), asbf(fa[b][r]), acc[r][ci]);
  };
  const int grow = rb64 * 64 + 16 * w + (lane & 15), gcol = 80 * cb + 4 * (lane >> 4);
  u2 xv[5];
  bp += 2048;
  issue(0);
#pragma unroll 1
  for (int it = 0; it < 8; ++it) {
    issue(1);
    K2_WAIT(0, 9);
    mm(0);
    issue(0);
    K2_WAIT(1, 9);
    mm(1);
  }
  {
    const bf16_t* xp = x + grow * 640 + gcol;
#pragma unroll
    for (int ci = 0; ci < 5; ++ci)
      asm volatile("global_load_dwordx2 %0, %1, off offset:%2" : "=v"(xv[ci]) : "v"(xp), "n"(ci * 32));
  }
  K2_WAIT(0, 5);
  mm(0);
#pragma unroll
  for (int r = 3; r >= 1; --r) {
    char* d = lds + ((((r + w) & 3) * 3 + r - 1) * 5) * 1024 + lane * 16;
#pragma unroll
    for (int q = 0; q < 5; ++q) *(f4*)(d + q * 1024) = acc[r][q];
  }
  LBAR();
  asm volatile("s_waitcnt vmcnt(0)" : "+v"(xv[0]), "+v"(xv[1]), "+v"(xv[2]), "+v"(xv[3]), "+v"(xv[4]) :: "memory");
#pragma unroll
  for (int q = 0; q < 5; ++q) {
    f4 s = acc[0][q];
#pragma unroll
    for (int t = 0; t < 3; ++t) s += *(const f4*)(lds + ((w * 3 + t) * 5 + q) * 1024 + lane * 16);
    u2 o;
    o[0] = pk2(s[0] + lo16(xv[q][0]), s[1] + hi16(xv[q][0]));
    o[1] = pk2(s[2] + lo16(xv[q][1]), s[3] + hi16(xv[q][1]));
    *(u2*)(out + grow * 640 + gcol + 16 * q) = o;
  }
}

extern "C" void kernel_launch(void* const* d_in, const int* in_sizes, int n_in, void* d_out, int out_size,
                              void* d_ws, size_t ws_size, hipStream_t stream) {
  const bf16_t* x = (const bf16_t*)d_in[0];
  const bf16_t* wqkv = (const bf16_t*)d_in[1];
  const bf16_t* wo = (const bf16_t*)d_in[2];
  const bf16_t* wg = (const bf16_t*)d_in[4];
  const bf16_t* wu = (const bf16_t*)d_in[5];
  const bf16_t* wd = (const bf16_t*)d_in[6];
  bf16_t* A2 = (bf16_t*)d_ws;
  bf16_t* W2f = A2 + 6 * 1024 * 1024;
  k1_kernel<<<256, 512, 0, stream>>>(x, wqkv, wg, wu, wo, wd, A2, W2f);
  k2_kernel<<<256, 256, 0, stream>>>(A2, W2f, x, (bf16_t*)d_out);
}
```
